```python
import torch

def _softmax(x, axis=-1):
    return torch.softmax(x, dim=axis)
def _gelu(x):
    return torch.nn.functional.gelu(x, approximate="tanh")
def _silu(x):
    return torch.nn.functional.silu(x)
def _sigmoid(x):
    return torch.sigmoid(x)
def _layernorm(x, axis=-1, eps=1e-5):
    m = x.mean(dim=axis, keepdim=True)
    v = x.var(dim=axis, keepdim=True, unbiased=False)
    return (x - m) / torch.sqrt(v + eps)
def _scatter_add(tgt, idx, src, axis=0):
    return tgt.clone().index_add_(axis, idx, src)
def _segment_sum(src, seg, n):
    out = torch.zeros((n,) + src.shape[1:], dtype=src.dtype)
    return out.index_add_(0, seg, src)
def _pad(x, pw):
    flat = tuple(v for pair in reversed(pw) for v in pair)
    return torch.nn.functional.pad(x, flat)

kernel_name = 'parallel_sum__spectral_fnet__dense_activation_relu__prompt'

def setup_inputs():

    _w = (lambda a: (lambda t: t.to(torch.bfloat16) if t.is_floating_point() else t)(torch.as_tensor(a)))
    def _A_setup():
        B, S, D = 1, 2048, 448
        ns = torch.arange(S)[:, None] * torch.arange(S)[None, :]
        nd = torch.arange(D)[:, None] * torch.arange(D)[None, :]
        Ws_re = torch.cos(-2.0 * torch.pi * ns / S).to(float)
        Ws_im = torch.sin(-2.0 * torch.pi * ns / S).to(float)
        Wd_re = torch.cos(-2.0 * torch.pi * nd / D).to(float)
        Wd_im = torch.sin(-2.0 * torch.pi * nd / D).to(float)
        return {
            "x": _w((lambda *s: torch.randn(*s, dtype=torch.bfloat16) * 0.05)(B, S, D)),
            "dft_seq_re": _w(Ws_re),
            "dft_seq_im": _w(Ws_im),
            "dft_hid_re": _w(Wd_re),
            "dft_hid_im": _w(Wd_im),
        }
    _A_in = _A_setup()

    def _B_setup():
        B, S, D, Dff = 1, 2048, 448, 1792
        return {
            "x": _w((lambda *s: torch.randn(*s, dtype=torch.bfloat16) * 0.05)(B, S, D)),
            "w1": _w((lambda *s: torch.randn(*s, dtype=torch.bfloat16) * 0.4)(D, Dff)),
            "w2": _w((lambda *s: torch.randn(*s, dtype=torch.bfloat16) * 0.4)(Dff, D)),
        }
    _B_in = _B_setup()

    return {
        "A_x": _A_in["x"],
        "A_dft_seq_re": _A_in["dft_seq_re"],
        "A_dft_seq_im": _A_in["dft_seq_im"],
        "A_dft_hid_re": _A_in["dft_hid_re"],
        "A_dft_hid_im": _A_in["dft_hid_im"],
        "B_x": _B_in["x"],
        "B_w1": _B_in["w1"],
        "B_w2": _B_in["w2"],
    }

def reference(A_x, A_dft_seq_re, A_dft_seq_im, A_dft_hid_re, A_dft_hid_im, B_x, B_w1, B_w2):
    def _A_ref(x, dft_seq_re, dft_seq_im, dft_hid_re, dft_hid_im):
        a = torch.einsum("bsd,ed->bse", x, dft_hid_re)
        b = torch.einsum("bsd,ed->bse", x, dft_hid_im)
        y = torch.einsum("ts,bse->bte", dft_seq_re, a) - torch.einsum("ts,bse->bte", dft_seq_im, b)
        return y

    def _B_ref(x, w1, w2):
        h = torch.einsum("bsd,df->bsf", x, w1)
        zero = torch.zeros_like(h)
        h = torch.maximum(h, zero)
        return torch.einsum("bsf,fd->bsd", h, w2)

    x = A_x
    xn = _layernorm(x, axis=-1)
    return x + _A_ref(xn, A_dft_seq_re, A_dft_seq_im, A_dft_hid_re, A_dft_hid_im) + _B_ref(xn, B_w1, B_w2)
```

```cpp
#include <hip/hip_runtime.h>
#include <hip/hip_bf16.h>
#include <stdint.h>
typedef __attribute__((ext_vector_type(8))) short bf16x8;
typedef __attribute__((ext_vector_type(4))) float f32x4;
typedef unsigned short u16;
constexpr int S=2048, D=448, F=1792;
__device__ __forceinline__ float bf2f(u16 v){ return __uint_as_float(((unsigned)v)<<16); }
__device__ __forceinline__ u16 f2bf(float f){ __hip_bfloat16 b = __float2bfloat16(f); return *(u16*)&b; }

__global__ void ln_kernel(const u16* x, u16* xn){
  int row = blockIdx.x*4 + (threadIdx.x>>6); int lane = threadIdx.x&63;
  float v[7]; float s=0;
  for(int i=0;i<7;i++){ v[i]=bf2f(x[row*D+lane+64*i]); s+=v[i]; }
  for(int o=32;o;o>>=1) s+=__shfl_xor(s,o);
  float m=s/D; float q=0;
  for(int i=0;i<7;i++){ float d=v[i]-m; q+=d*d; }
  for(int o=32;o;o>>=1) q+=__shfl_xor(q,o);
  float r=rsqrtf(q/D+1e-5f);
  for(int i=0;i<7;i++) xn[row*D+lane+64*i]=f2bf((v[i]-m)*r);
}
template<int MODE>
__global__ void gemm_naive(const u16* A,int lda,const u16* B,int sbk,int sbn,int K,int N,u16* Cb,float* Cf,int ldc,float sign,const u16* x){
  int w = blockIdx.x*4 + (threadIdx.x>>6); int lane=threadIdx.x&63;
  int tn = N/16; int tm = w/tn; int n0=(w%tn)*16, m0=tm*16;
  f32x4 acc={0,0,0,0};
  int r=lane&15,h=lane>>4;
  for(int k=0;k<K;k+=32){
    bf16x8 a=*(const bf16x8*)(A+(size_t)(m0+r)*lda+k+h*8);
    bf16x8 b;
    for(int j=0;j<8;j++) b[j]=B[(size_t)(k+h*8+j)*sbk+(size_t)(n0+r)*sbn];
    acc=__builtin_amdgcn_mfma_f32_16x16x32_bf16(a,b,acc,0,0,0);
  }
  for(int j=0;j<4;j++){
    int row=m0+h*4+j,col=n0+r; size_t o=(size_t)row*ldc+col;
    if(MODE==0) Cb[o]=f2bf(acc[j]);
    if(MODE==1) Cb[o]=f2bf(fmaxf(acc[j],0.f));
    if(MODE==2) Cf[o]=acc[j];
    if(MODE==3) Cf[o]+=sign*acc[j];
    if(MODE==4) Cb[o]=f2bf(bf2f(x[o])+Cf[o]+acc[j]);
  }
}
extern "C" void kernel_launch(void* const* d_in, const int* in_sizes, int n_in,
                              void* d_out, int out_size, void* d_ws, size_t ws_size,
                              hipStream_t stream) {
  const u16* x=(const u16*)d_in[0]; const u16* wsr=(const u16*)d_in[1]; const u16* wsi=(const u16*)d_in[2];
  const u16* wdr=(const u16*)d_in[3]; const u16* wdi=(const u16*)d_in[4];
  const u16* w1=(const u16*)d_in[6]; const u16* w2=(const u16*)d_in[7];
  u16* out=(u16*)d_out;
  char* ws=(char*)d_ws;
  u16* xn=(u16*)ws; u16* a=(u16*)(ws+(4<<20)); u16* b=(u16*)(ws+(8<<20)); u16* h=(u16*)(ws+(12<<20)); float* acc=(float*)(ws+(24<<20));
  ln_kernel<<<S/4,256,0,stream>>>(x,xn);
  gemm_naive<0><<<(S/16)*(D/16)/4,256,0,stream>>>(xn,D,wdr,1,D,D,D,a,nullptr,D,1.f,nullptr);
  gemm_naive<0><<<(S/16)*(D/16)/4,256,0,stream>>>(xn,D,wdi,1,D,D,D,b,nullptr,D,1.f,nullptr);
  gemm_naive<1><<<(S/16)*(F/16)/4,256,0,stream>>>(xn,D,w1,F,1,D,F,h,nullptr,F,1.f,nullptr);
  gemm_naive<2><<<(S/16)*(D/16)/4,256,0,stream>>>(wsr,S,a,D,1,S,D,nullptr,acc,D,1.f,nullptr);
  gemm_naive<3><<<(S/16)*(D/16)/4,256,0,stream>>>(wsi,S,b,D,1,S,D,nullptr,acc,D,-1.f,nullptr);
  gemm_naive<4><<<(S/16)*(D/16)/4,256,0,stream>>>(h,F,w2,D,1,F,D,out,acc,D,1.f,x);
}
```

```cpp
#include <hip/hip_runtime.h>
#include <stdint.h>

#define LAS __attribute__((address_space(3)))
#define GAS __attribute__((address_space(1)))
#define DI __device__ __forceinline__
typedef __attribute__((ext_vector_type(8))) __bf16 bf16x8;
typedef __attribute__((ext_vector_type(4))) __bf16 bf16x4;
typedef __attribute__((ext_vector_type(2))) __bf16 bf16x2;
typedef __attribute__((ext_vector_type(2))) short s16x2;
typedef __attribute__((ext_vector_type(4))) float f32x4;
typedef __attribute__((ext_vector_type(2))) float f32x2;
typedef __attribute__((ext_vector_type(4))) unsigned u32x4;
typedef __attribute__((ext_vector_type(2))) unsigned u32x2;
typedef unsigned long long u64;
typedef __amdgpu_buffer_rsrc_t rsrc_t;

constexpr int S_ = 2048, D_ = 448, F_ = 1792;
constexpr unsigned WS_EPOCH = 0;
constexpr unsigned WS_FAB = 4096;
constexpr unsigned WS_FW2 = 8192;
constexpr unsigned WS_FH = 12288;
constexpr unsigned WS_FP = 16384;
constexpr unsigned WS_STAMP = 65536;
constexpr unsigned WS_AB = 1u << 20;
constexpr unsigned WS_W2 = 5u << 20;
constexpr unsigned WS_H = 7u << 20;
constexpr unsigned WS_P = 15u << 20;
constexpr unsigned WS_END = 23u << 20;
constexpr int NA = 6, NB = 4, ASZ = 16384, BSZ = 14336;
constexpr int L_A = 0, L_B = NA * ASZ, L_CTL = L_B + NB * BSZ;
constexpr int C_FULL = L_CTL;
constexpr int C_FREEA = L_CTL + 16;
constexpr int C_FREEB = L_CTL + 32;
constexpr int C_CS = L_CTL + 48;
constexpr int C_ARR = L_CTL + 64;
constexpr int C_OWN = L_CTL + 72;
constexpr int C_EP = L_CTL + 80;
constexpr int L_ST = L_CTL + 128;
constexpr int L_CS = L_ST + 1024;
constexpr int LDS_BYTES = L_CS + 512;
constexpr int NITA = 37, NITB = 30;
constexpr unsigned SPIN_LDS = 1u << 18, SPIN_GLB = 1u << 14;

#define AUX_WS 0
#define JA 8
#define JB 0

struct Params {
  const char* x; const char* wsr; const char* wsi; const char* wdr; const char* wdi; const char* w1; const char* w2;
  char* out; char* ws;
};

DI rsrc_t mk(const void* p, unsigned n) { return __builtin_amdgcn_make_buffer_rsrc((void*)p, 0, n, 0x00020000); }
DI unsigned rfl(unsigned v) { return __builtin_amdgcn_readfirstlane(v); }
template <int N> DI void waitv() { asm volatile("s_waitcnt vmcnt(%0)" ::"n"(N) : "memory"); }
DI void lds_st(unsigned a, unsigned v) { asm volatile("ds_write_b32 %0, %1" ::"v"(a), "v"(v) : "memory"); }
DI u32x4 lds_ld4_wait(unsigned a) { u32x4 v; asm volatile("ds_read_b128 %0, %1\n\ts_waitcnt lgkmcnt(0)" : "=&v"(v) : "v"(a) : "memory"); return v; }
DI unsigned min4(u32x4 v) { return min(min(v.x, v.y), min(v.z, v.w)); }
DI unsigned pk(float a, float b) { f32x2 f = {a, b}; return __builtin_bit_cast(unsigned, __builtin_convertvector(f, bf16x2)); }
DI unsigned relu2(unsigned v) { s16x2 t = __builtin_bit_cast(s16x2, v), zz = {0, 0}; return __builtin_bit_cast(unsigned, __builtin_elementwise_max(t, zz)); }
DI float lo16(unsigned v) { return __builtin_bit_cast(float, v << 16); }
DI float hi16(unsigned v) { return __builtin_bit_cast(float, v & 0xffff0000u); }
DI f32x4 mma(bf16x8 a, bf16x8 b, f32x4 c) { return __builtin_amdgcn_mfma_f32_16x16x32_bf16(a, b, c, 0, 0, 0); }
#define CBAR() asm volatile("" ::: "memory")
#define SCHED() __builtin_amdgcn_sched_barrier(0)
#define STAMP(k) do {} while (0)
#define CSTAMP(k) do {} while (0)

template <int AUX> DI void g4(rsrc_t r, LAS char* dst, unsigned v0, unsigned v1, unsigned v2, unsigned v3, unsigned so) {
  __builtin_amdgcn_raw_ptr_buffer_load_lds(r, (LAS void*)dst, 16, v0, so, 0, AUX);
  __builtin_amdgcn_raw_ptr_buffer_load_lds(r, (LAS void*)dst, 16, v1, so, 1024, AUX);
  __builtin_amdgcn_raw_ptr_buffer_load_lds(r, (LAS void*)dst, 16, v2, so, 2048, AUX);
  __builtin_amdgcn_raw_ptr_buffer_load_lds(r, (LAS void*)dst, 16, v3, so, 3072, AUX);
}
template <int AUX> DI void g2(rsrc_t r, LAS char* dst, unsigned v0, unsigned v1, unsigned so) {
  __builtin_amdgcn_raw_ptr_buffer_load_lds(r, (LAS void*)dst, 16, v0, so, 0, AUX);
  __builtin_amdgcn_raw_ptr_buffer_load_lds(r, (LAS void*)dst, 16, v1, so, 1024, AUX);
}

struct Ring {
  unsigned fullAddr, freeAddr, pub, fr; bool dead;
  DI void publish(unsigned n) { if (n > pub) { pub = n; lds_st(fullAddr, n); } }
  template <int PREV> DI void space(unsigned need, unsigned n) {
    if (fr >= need) return;
    fr = rfl(min4(lds_ld4_wait(freeAddr)));
    if (fr >= need) return;
    waitv<PREV>(); publish(n - 1);
    fr = rfl(min4(lds_ld4_wait(freeAddr)));
    if (fr >= need) return;
    waitv<0>(); publish(n);
    for (unsigned sp = 0; !dead; ++sp) {
      fr = rfl(min4(lds_ld4_wait(freeAddr)));
      if (fr >= need) return;
      if (sp > SPIN_LDS) dead = true;
    }
  }
};
DI u64 get_epoch(bool& dead) {
  u32x4 e;
  for (unsigned sp = 0;; ++sp) { e = lds_ld4_wait(C_EP); if (rfl(e.z) != 0u || dead) break; if (sp > SPIN_LDS) dead = true; }
  return ((u64)rfl(e.y) << 32) | rfl(e.x);
}
DI void poll_flags(const char* ws, unsigned off, bool on, u64 ep, bool& dead) {
  for (unsigned sp = 0; !dead; ++sp) {
    u64 v = __hip_atomic_load((const GAS u64*)(ws + off), __ATOMIC_RELAXED, __HIP_MEMORY_SCOPE_AGENT);
    if (__builtin_amdgcn_ballot_w64(on && v != ep) == 0ull) return;
    __builtin_amdgcn_s_sleep(2);
    if (sp > SPIN_GLB) dead = true;
  }
}
DI void st_flag(char* ws, unsigned off, u64 ep) { __hip_atomic_store((GAS u64*)(ws + off), ep, __ATOMIC_RELAXED, __HIP_MEMORY_SCOPE_AGENT); }

__global__ __launch_bounds__(384, 2) void fnet_kernel(Params p) {
  extern __shared__ __attribute__((aligned(16))) char shm_[];
  LAS char* L = (LAS char*)shm_;
  const int tid = threadIdx.x, lane = tid & 63;
  const int wave = rfl(tid >> 6);
  const int b = blockIdx.x, xcd = b & 7, rh = xcd & 1, q = xcd >> 1, r = b >> 5, c = (b >> 3) & 3, rb = rh * 8 + r;
  const bool idle = (q & 1) && c == 3;
  const int u = (7 * q + 1) / 2 + c, id = rb * 2 + (q >> 1);
  const int rot7 = (2 * c + r) % 7, rot16 = (4 * c + 2 * r) & 15;
  const int mat = q >> 1, sh = q & 1;
  STAMP(0); CSTAMP(0);
  if (tid < 32) ((LAS unsigned*)(L + L_CTL))[tid] = 0u;
  asm volatile("s_waitcnt lgkmcnt(0)" ::: "memory");
  __builtin_amdgcn_s_barrier();
  const rsrc_t rw = mk(p.ws, WS_END);

  if (wave == 4) {
    __builtin_amdgcn_s_setprio(3);
    Ring R{C_FULL, C_FREEA, 0, 0, false};
    const unsigned ro = lane >> 3, ch = (lane & 7) ^ ((ro >> 1) & 1);
    unsigned n = 0, slot = 0;
    auto item = [&](auto aux, rsrc_t rs, unsigned v0, unsigned v1, unsigned v2, unsigned v3, unsigned so, unsigned st) {
      constexpr int AX = decltype(aux)::value;
      if (n >= NA) R.space<16>(n - NA + 1, n);
      LAS char* d = L + L_A + slot * ASZ;
      if (JA == 0) { waitv<16>(); if (n) R.publish(n - 1); }
      g4<AX>(rs, d, v0, v1, v2, v3, so);
      g4<AX>(rs, d + 4096, v0, v1, v2, v3, so + st);
      if (JA == 8) { waitv<24>(); if (n) R.publish(n - 1); }
      g4<AX>(rs, d + 8192, v0, v1, v2, v3, so + 2 * st);
      g4<AX>(rs, d + 12288, v0, v1, v2, v3, so + 3 * st);
      if (JA == 16) { waitv<32>(); if (n) R.publish(n - 1); }
      ++n; slot = slot == NA - 1 ? 0 : slot + 1;
    };
    std::integral_constant<int, 0> a0; std::integral_constant<int, AUX_WS> aws;
    if (!idle) {
      { const rsrc_t rs = mk(p.x, S_ * D_ * 2);
        const unsigned v0 = ro * 896 + ch * 16, v1 = (8 + ro) * 896 + (ch ^ 2) * 16 - 1024, v2 = (16 + ro) * 896 + (ch ^ 4) * 16 - 2048, v3 = (24 + ro) * 896 + (ch ^ 6) * 16 - 3072;
        unsigned kc = rot7;
#pragma unroll 1
        for (int s = 0; s < 7; ++s) { item(a0, rs, v0, v1, v2, v3, rb * 128 * 896 + kc * 128, 32 * 896); kc = kc == 6 ? 0 : kc + 1; } }
      { const rsrc_t rs = mk(p.w1, D_ * F_ * 2);
        const unsigned qq = lane >> 4, c0 = (lane & 15) ^ (qq << 1);
        const unsigned v0 = qq * 3584 + c0 * 16, v1 = (4 + qq) * 3584 + c0 * 16 - 1024, v2 = (8 + qq) * 3584 + (c0 ^ 8) * 16 - 2048, v3 = (12 + qq) * 3584 + (c0 ^ 8) * 16 - 3072;
        unsigned kc = rot7;
#pragma unroll 1
        for (int s = 0; s < 7; ++s) { item(a0, rs, v0, v1, v2, v3, kc * 64 * 3584 + u * 256, 16 * 3584); kc = kc == 6 ? 0 : kc + 1; } }
    } else {
      if (id < 28) {
        const rsrc_t rs = mk(p.w2, D_ * F_ * 2);
        const unsigned v = lane * 16;
#pragma unroll 1
        for (int i = 0; i < 14; ++i) g4<0>(rs, L + L_A + i * 4096, v, v, v, v, id * 57344 + i * 4096);
        waitv<0>();
      }
      R.publish(14); n = 14; slot = 14 % NA;
    }
    STAMP(1);
    { const rsrc_t rs = mk(mat ? p.wsi : p.wsr, S_ * S_ * 2);
      const unsigned v0 = ro * 4096 + ch * 16, v1 = (8 + ro) * 4096 + (ch ^ 2) * 16 - 1024, v2 = (16 + ro) * 4096 + (ch ^ 4) * 16 - 2048, v3 = (24 + ro) * 4096 + (ch ^ 6) * 16 - 3072;
      unsigned kc = rot16;
#pragma unroll 1
      for (int s = 0; s < 16; ++s) { item(aws, rs, v0, v1, v2, v3, rb * 128 * 4096 + sh * 2048 + kc * 128, 32 * 4096); kc = (kc + 1) & 15; } }
    STAMP(2);
    waitv<16>(); R.publish(n - 1); waitv<0>(); R.publish(n);
    { const u64 ep = get_epoch(R.dead);
      poll_flags(p.ws, WS_FH + (rb * 16 + (14 * q) / 4 + (lane & 3)) * 8, true, ep, R.dead);
      __builtin_amdgcn_fence(__ATOMIC_ACQUIRE, "agent");
      STAMP(3);
      const unsigned v = lane * 16;
      unsigned kc = rot7;
#pragma unroll 1
      for (int s = 0; s < 7; ++s) { item(a0, rw, v, v, v, v, WS_H + (rb * 28 + 7 * q + kc) * 16384, 4096); kc = kc == 6 ? 0 : kc + 1; } }
    waitv<16>(); R.publish(n - 1); waitv<0>(); R.publish(n);
    STAMP(4);
    return;
  }
  if (wave == 5) {
    __builtin_amdgcn_s_setprio(3);
    Ring R{C_FULL + 4, C_FREEB, 0, 0, false};
    unsigned n = 0, slot = 0;
    if (!idle) {
      const unsigned ro = lane >> 3, ch = (lane & 7) ^ ((ro >> 1) & 1);
      const rsrc_t rs = mk(u >= 7 ? p.wdi : p.wdr, D_ * D_ * 2);
      const unsigned v0 = ro * 896 + ch * 16, v1 = (8 + ro) * 896 + (ch ^ 2) * 16 - 1024, v2 = (16 + ro) * 896 + (ch ^ 4) * 16 - 2048, v3 = (24 + ro) * 896 + (ch ^ 6) * 16 - 3072;
      const unsigned e0 = (u % 7) * 64 * 896;
      unsigned kc = rot7;
#pragma unroll 1
      for (int s = 0; s < 7; ++s) {
        if (n >= NB) R.space<8>(n - NB + 1, n);
        LAS char* d = L + L_B + slot * BSZ;
        g4<0>(rs, d, v0, v1, v2, v3, e0 + kc * 128);
        g4<0>(rs, d + 4096, v0, v1, v2, v3, e0 + kc * 128 + 32 * 896);
        waitv<8>(); R.publish(n);
        ++n; slot = (slot + 1) & 3; kc = kc == 6 ? 0 : kc + 1;
      }
      waitv<0>(); R.publish(n);
    } else { n = 7; slot = 3; R.publish(7); }
    STAMP(1);
    const u64 ep = get_epoch(R.dead);
    poll_flags(p.ws, lane < 56 ? WS_FAB + (q * 64 + lane) * 8 : WS_FW2 + (7 * q + (lane - 56) % 7) * 8, true, ep, R.dead);
    __builtin_amdgcn_fence(__ATOMIC_ACQUIRE, "agent");
    STAMP(2);
    const unsigned v = lane * 16;
    auto item = [&](unsigned so) {
      R.space<14>(n - NB + 1, n);
      LAS char* d = L + L_B + slot * BSZ;
      if (JB == 0) { waitv<14>(); R.publish(n - 1); }
      g4<0>(rw, d, v, v, v, v, so);
      g4<0>(rw, d + 4096, v, v, v, v, so + 4096);
      if (JB == 8) { waitv<22>(); R.publish(n - 1); }
      g4<0>(rw, d + 8192, v, v, v, v, so + 8192);
      g2<0>(rw, d + 12288, v, v, so + 12288);
      ++n; slot = (slot + 1) & 3;
    };
    { unsigned kc = rot16;
#pragma unroll 1
      for (int s = 0; s < 16; ++s) { item(WS_AB + ((mat * 4 + c) * 32 + sh * 16 + kc) * BSZ); kc = (kc + 1) & 15; } }
    STAMP(3);
    { unsigned kc = rot7;
#pragma unroll 1
      for (int s = 0; s < 7; ++s) { item(WS_W2 + (c * 28 + 7 * q + kc) * BSZ); kc = kc == 6 ? 0 : kc + 1; } }
    waitv<14>(); R.publish(n - 1); waitv<0>(); R.publish(n);
    STAMP(4);
    if (b == 0) {
      for (int i = 0; i < 4; ++i) poll_flags(p.ws, WS_FAB + (i * 64 + (lane < 56 ? lane : 0)) * 8, true, ep, R.dead);
      poll_flags(p.ws, WS_FW2 + (lane & 31) * 8, true, ep, R.dead);
      if (lane == 0) st_flag(p.ws, WS_EPOCH, ep);
    }
    return;
  }

  const int w = wave, i16 = lane & 15, g = lane >> 4;
  bool dead = false;
  unsigned fa = 0, fb = 0;
  u64 ep;
  { const u64 w0 = __hip_atomic_load((const GAS u64*)(p.ws + WS_EPOCH), __ATOMIC_RELAXED, __HIP_MEMORY_SCOPE_AGENT);
    ep = (((u64)rfl((unsigned)(w0 >> 32)) << 32) | rfl((unsigned)w0)) * 6364136223846793005ull + 1442695040888963407ull;
    if (w == 0) { lds_st(C_EP, (unsigned)ep); lds_st(C_EP + 4, (unsigned)(ep >> 32)); lds_st(C_EP + 8, 1u); } }
  auto wait_full = [&](unsigned na, unsigned nb) {
    for (unsigned sp = 0; (fa < na || fb < nb) && !dead; ++sp) {
      u64 f = __hip_atomic_load((LAS u64*)(L + C_FULL), __ATOMIC_RELAXED, __HIP_MEMORY_SCOPE_WORKGROUP);
      fa = rfl((unsigned)f); fb = rfl((unsigned)(f >> 32));
      if (sp > SPIN_LDS) dead = true;
    }
    CBAR();
  };
  auto arrive = [&](int ctr, unsigned flagoff) {
    waitv<0>();
    unsigned old = 0;
    if (lane == 0) old = __hip_atomic_fetch_add((LAS unsigned*)(L + C_ARR + 4 * ctr), 1u, __ATOMIC_RELAXED, __HIP_MEMORY_SCOPE_WORKGROUP);
    if (rfl(old) == 3u && lane == 0) st_flag(p.ws, flagoff, ep);
  };
  const unsigned fsw = ((i16 >> 2) << 1) | ((i16 >> 1) & 1);
  const unsigned rowpart = (i16 >> 2) * 1024 + (i16 & 3) * 128;
  const unsigned ro0 = rowpart + ((g ^ fsw) << 4), ro1 = rowpart + (((4 + g) ^ fsw) << 4);
  const bf16x8 ones = {1, 1, 1, 1, 1, 1, 1, 1};
  const f32x4 z4 = {0.f, 0.f, 0.f, 0.f};

  if (!idle) {
    bf16x8 xf[2][14];
    f32x2 stl[2];
    {
      f32x4 acc[4][2], cs[4], gr[2], sm[2];
#pragma unroll
      for (int n = 0; n < 4; ++n) { acc[n][0] = z4; acc[n][1] = z4; cs[n] = z4; }
      gr[0] = gr[1] = sm[0] = sm[1] = z4;
#pragma unroll
      for (int s = 0; s < 7; ++s) {
        wait_full(s + 1, s + 1);
        if (s == 0) STAMP(1);
        LAS char* pa = L + L_A + (s % NA) * ASZ + w * 4096;
        LAS char* pb = L + L_B + (s % NB) * BSZ;
        bf16x8 wf[2][4];
#pragma unroll
        for (int kk = 0; kk < 2; ++kk) {
          const unsigned o = kk ? ro1 : ro0;
          xf[0][2 * s + kk] = *(LAS bf16x8*)(pa + o);
          xf[1][2 * s + kk] = *(LAS bf16x8*)(pa + o + 512);
#pragma unroll
          for (int n = 0; n < 4; ++n) wf[kk][n] = *(LAS bf16x8*)(pb + o + (n >> 1) * 4096 + (n & 1) * 512);
        }
        lds_st(C_FREEA + 4 * w, s + 1); lds_st(C_FREEB + 4 * w, s + 1);
#pragma unroll
        for (int kk = 0; kk < 2; ++kk) {
#pragma unroll
          for (int m = 0; m < 2; ++m) { gr[m] = mma(xf[m][2 * s + kk], xf[m][2 * s + kk], gr[m]); sm[m] = mma(xf[m][2 * s + kk], ones, sm[m]); }
#pragma unroll
          for (int n = 0; n < 4; ++n) {
            acc[n][0] = mma(xf[0][2 * s + kk], wf[kk][n], acc[n][0]);
            acc[n][1] = mma(xf[1][2 * s + kk], wf[kk][n], acc[n][1]);
            cs[n] = mma(ones, wf[kk][n], cs[n]);
          }
        }
      }
      STAMP(2);
      LAS f32x2* st = (LAS f32x2*)(L + L_ST) + w * 32;
#pragma unroll
      for (int m = 0; m < 2; ++m) {
        const int e = lane & 3;
        const float sq = e == 0 ? gr[m][0] : e == 1 ? gr[m][1] : e == 2 ? gr[m][2] : gr[m][3];
        const float su = e == 0 ? sm[m][0] : e == 1 ? sm[m][1] : e == 2 ? sm[m][2] : sm[m][3];
        const float mu = su * (1.f / D_), var = fmaxf(sq * (1.f / D_) - mu * mu, 0.f), rs = 1.f / sqrtf(var + 1e-5f);
        if ((i16 >> 2) == g) { f32x2 t = {rs, -mu * rs}; st[m * 16 + i16] = t; }
      }
      CBAR();
      const float sg = u >= 7 ? -1.f : 1.f;
      f32x2 sr[2][4];
#pragma unroll
      for (int m = 0; m < 2; ++m) {
        stl[m] = st[m * 16 + i16];
#pragma unroll
        for (int e = 0; e < 4; ++e) sr[m][e] = st[m * 16 + 4 * g + e] * sg;
      }
#pragma unroll
      for (int n = 0; n < 4; ++n) {
        float v[2][4];
#pragma unroll
        for (int m = 0; m < 2; ++m)
#pragma unroll
          for (int e = 0; e < 4; ++e) v[m][e] = fmaf(cs[n][0], sr[m][e].y, acc[n][m][e] * sr[m][e].x);
        u32x4 o = {pk(v[0][0], v[0][1]), pk(v[0][2], v[0][3]), pk(v[1][0], v[1][1]), pk(v[1][2], v[1][3])};
        const int NT = (u % 7) * 4 + n;
        __builtin_amdgcn_raw_buffer_store_b128(o, rw, lane * 16, WS_AB + ((((u / 7) * 4 + NT / 7) * 64 + rb * 4 + w) * 7 + NT % 7) * 1024, 16);
      }
      STAMP(3);
    }
    {
      f32x4 acc[8][2], cs[2];
#pragma unroll
      for (int n = 0; n < 8; ++n) { acc[n][0] = z4; acc[n][1] = z4; }
      cs[0] = cs[1] = z4;
      const unsigned tq = i16 >> 2, tp = i16 & 3;
      const unsigned tb = (8 * g + tq) * 256 + ((((tq | ((g & 1) << 2)) ^ (w << 1))) << 5) + tp * 8;
      bf16x8 wf[2][4];
      auto rd = [&](int hs, bf16x8 (&f)[4]) {
        const int s = hs >> 2, kk = (hs >> 1) & 1, hf = hs & 1;
        LAS char* pa = L + L_A + ((7 + s) % NA) * ASZ + kk * 8192;
#pragma unroll
        for (int n = 0; n < 4; ++n) {
          const unsigned o = tb ^ ((hf * 4 + n) << 5);
          bf16x4 lo = __builtin_amdgcn_ds_read_tr16_b64_v4bf16((LAS bf16x4*)(pa + o));
          bf16x4 hi = __builtin_amdgcn_ds_read_tr16_b64_v4bf16((LAS bf16x4*)(pa + o + 1024));
          f[n] = __builtin_shufflevector(lo, hi, 0, 1, 2, 3, 4, 5, 6, 7);
        }
      };
      wait_full(8, 0);
      rd(0, wf[0]);
#pragma unroll
      for (int hs = 0; hs < 28; ++hs) {
        if ((hs & 3) == 3 && hs < 27) {
          if (hs == 15) arrive(0, WS_FAB + (((u / 7) * 2 + rh) * 64 + r * 7 + u % 7) * 8);
          wait_full(9 + (hs >> 2), 0);
        }
        SCHED();
        if (hs < 27) rd(hs + 1, wf[(hs + 1) & 1]);
        const int ks = hs >> 1, hf = hs & 1;
#pragma unroll
        for (int n = 0; n < 4; ++n) {
          acc[hf * 4 + n][0] = mma(wf[hs & 1][n], xf[0][ks], acc[hf * 4 + n][0]);
          acc[hf * 4 + n][1] = mma(wf[hs & 1][n], xf[1][ks], acc[hf * 4 + n][1]);
          if (hf == 0 && n < 2) cs[n] = mma(wf[hs & 1][n], ones, cs[n]);
        }
#pragma unroll
        for (int k = 0; k < 8; ++k) { __builtin_amdgcn_sched_group_barrier(0x008, 1, 0); __builtin_amdgcn_sched_group_barrier(0x100, 1, 0); }
        SCHED();
        if ((hs & 3) == 2) lds_st(C_FREEA + 4 * w, 8 + (hs >> 2));
      }
      STAMP(4);
      LAS f32x4* ct = (LAS f32x4*)(L + L_CS);
      if (i16 == 0) { ct[w * 8 + g] = cs[0]; ct[w * 8 + 4 + g] = cs[1]; }
      lds_st(C_CS + 4 * w, 1u);
#pragma unroll
      for (int n = 0; n < 8; ++n) { acc[n][0] *= stl[0].x; acc[n][1] *= stl[1].x; }
      for (unsigned sp = 0; !dead; ++sp) { if (rfl(min4(lds_ld4_wait(C_CS))) != 0u) break; if (sp > SPIN_LDS) dead = true; }
#pragma unroll
      for (int pr = 0; pr < 4; ++pr) {
        const f32x4 c0 = ct[(pr ^ w) * 8 + g], c1 = ct[(pr ^ w) * 8 + 4 + g];
        const int kf = u * 4 + (pr ^ w);
#pragma unroll
        for (int m = 0; m < 2; ++m) {
          const f32x4 v0 = c0 * stl[m].y + acc[2 * pr][m], v1 = c1 * stl[m].y + acc[2 * pr + 1][m];
          u32x4 o = {relu2(pk(v0[0], v0[1])), relu2(pk(v0[2], v0[3])), relu2(pk(v1[0], v1[1])), relu2(pk(v1[2], v1[3]))};
          __builtin_amdgcn_raw_buffer_store_b128(o, rw, (lane >> 5) * 1024 + (lane & 31) * 16, WS_H + (rb * 28 + (kf >> 1)) * 16384 + w * 4096 + (kf & 1) * 2048 + m * 512, 16);
        }
      }
      STAMP(5);
    }
  } else {
    wait_full(14, 0);
    if (id < 28) {
      const unsigned tq = i16 >> 2, tp = i16 & 3;
#pragma unroll 2
      for (int j = 0; j < 14; ++j) {
        const int fi = w * 14 + j, kl = fi / 28, NT = fi % 28;
        LAS char* pa = L + L_A + (kl * 32 + 4 * g + tq) * 896 + (NT >> 1) * 64 + tp * 16 + (NT & 1) * 8;
        bf16x4 lo = __builtin_amdgcn_ds_read_tr16_b64_v4bf16((LAS bf16x4*)pa);
        bf16x4 hi = __builtin_amdgcn_ds_read_tr16_b64_v4bf16((LAS bf16x4*)(pa + 16 * 896));
        bf16x8 f = __builtin_shufflevector(lo, hi, 0, 1, 2, 3, 4, 5, 6, 7);
        __builtin_amdgcn_raw_buffer_store_b128(__builtin_bit_cast(u32x4, f), rw, lane * 16, WS_W2 + (((NT / 7) * 56 + id * 2 + kl) * 7 + NT % 7) * 1024, 16);
      }
    }
    lds_st(C_FREEA + 4 * w, 14); lds_st(C_FREEB + 4 * w, 7);
    arrive(0, WS_FW2 + id * 8);
  }

  f32x4 acc[7][2];
#pragma unroll
  for (int n = 0; n < 7; ++n) { acc[n][0] = z4; acc[n][1] = z4; }
  {
    bf16x8 a0[2], b0[7], a1[2], b1[7];
    unsigned ao0 = w * 4096 + ro0, ao1 = w * 4096 + ro1;
    const unsigned bo = L_B + lane * 16;
    unsigned sa = (14 % NA) * ASZ, sb = (7 % NB) * BSZ;
    wait_full(15, 8);
    STAMP(6);
    a0[0] = *(LAS bf16x8*)(L + sa + ao0); a0[1] = *(LAS bf16x8*)(L + sa + ao0 + 512);
#pragma unroll
    for (int n = 0; n < 7; ++n) b0[n] = *(LAS bf16x8*)(L + sb + bo + n * 1024);
#pragma unroll 1
    for (int s = 0; s < 23; ++s) {
      SCHED();
      a1[0] = *(LAS bf16x8*)(L + sa + ao1); a1[1] = *(LAS bf16x8*)(L + sa + ao1 + 512);
#pragma unroll
      for (int n = 0; n < 7; ++n) b1[n] = *(LAS bf16x8*)(L + sb + bo + 7168 + n * 1024);
      u64 f = __hip_atomic_load((LAS u64*)(L + C_FULL), __ATOMIC_RELAXED, __HIP_MEMORY_SCOPE_WORKGROUP);
#pragma unroll
      for (int n = 0; n < 7; ++n) { acc[n][0] = mma(b0[n], a0[0], acc[n][0]); acc[n][1] = mma(b0[n], a0[1], acc[n][1]); }
#pragma unroll
      for (int k = 0; k < 10; ++k) { __builtin_amdgcn_sched_group_barrier(0x008, 1, 0); __builtin_amdgcn_sched_group_barrier(0x100, 1, 0); }
      SCHED();
      lds_st(C_FREEA + 4 * w, 15 + s); lds_st(C_FREEB + 4 * w, 8 + s);
      fa = rfl((unsigned)f); fb = rfl((unsigned)(f >> 32));
      if (s == 4 && !idle) arrive(1, WS_FH + (rb * 16 + u) * 8);
      if (s == 15) { ao0 = w * 4096 + (lane >> 5) * 1024 + (lane & 31) * 16; ao1 = ao0 + 2048; STAMP(7); }
      sa = sa == (NA - 1) * ASZ ? 0 : sa + ASZ; sb = sb == (NB - 1) * BSZ ? 0 : sb + BSZ;
      wait_full(min(16 + s, NITA), min(9 + s, NITB));
      SCHED();
      a0[0] = *(LAS bf16x8*)(L + sa + ao0); a0[1] = *(LAS bf16x8*)(L + sa + ao0 + 512);
#pragma unroll
      for (int n = 0; n < 7; ++n) b0[n] = *(LAS bf16x8*)(L + sb + bo + n * 1024);
#pragma unroll
      for (int n = 0; n < 7; ++n) { acc[n][0] = mma(b1[n], a1[0], acc[n][0]); acc[n][1] = mma(b1[n], a1[1], acc[n][1]); }
#pragma unroll
      for (int k = 0; k < 9; ++k) { __builtin_amdgcn_sched_group_barrier(0x008, 1, 0); __builtin_amdgcn_sched_group_barrier(0x100, 1, 0); }
      SCHED();
    }
  }
  STAMP(8);
  const int n0 = w < 3 ? 2 * w + (c & 1) : ((c & 1) ? 0 : 6), n1 = w < 3 ? n0 + 1 : n0;
  const int NT0 = 7 * c + n0;
  const unsigned xo = (rb * 128 + q * 32 + 8 * (i16 >> 2) + (i16 & 3)) * 896 + (NT0 >> 1) * 64 + g * 16;
  const rsrc_t rx = mk(p.x, S_ * D_ * 2);
  u32x4 xr[2];
  xr[0] = __builtin_amdgcn_raw_buffer_load_b128(rx, xo, 0, 0);
  xr[1] = __builtin_amdgcn_raw_buffer_load_b128(rx, xo + 4 * 896, 0, 0);
  if (w != q) {
    const int dest = (b & ~6) | (w << 1);
#pragma unroll
    for (int n = 0; n < 7; ++n) {
      u32x4 o = {pk(acc[n][0][0], acc[n][0][1]), pk(acc[n][0][2], acc[n][0][3]), pk(acc[n][1][0], acc[n][1][1]), pk(acc[n][1][2], acc[n][1][3])};
      __builtin_amdgcn_raw_buffer_store_b128(o, rw, lane * 16, WS_P + ((dest * 4 + q) * 7 + n) * 1024, 16);
    }
    waitv<0>();
    if (lane == 0) st_flag(p.ws, WS_FP + (dest * 16 + q) * 8, ep);
  } else {
#pragma unroll
    for (int n = 0; n < 7; ++n)
#pragma unroll
      for (int m = 0; m < 2; ++m) *(LAS f32x4*)(L + L_A + ((2 * n + m) >> 2) * ASZ + q * 4096 + ((2 * n + m) & 3) * 1024 + lane * 16) = acc[n][m];
    lds_st(C_OWN, 1u);
  }
  STAMP(9);
  for (unsigned sp = 0; !dead; ++sp) { if (rfl(lds_ld4_wait(C_ARR).z) != 0u) break; if (sp > SPIN_LDS) dead = true; }
  f32x4 o0[2], o1[2];
#pragma unroll
  for (int m = 0; m < 2; ++m) {
    o0[m] = *(LAS f32x4*)(L + L_A + ((2 * n0 + m) >> 2) * ASZ + q * 4096 + ((2 * n0 + m) & 3) * 1024 + lane * 16);
    o1[m] = *(LAS f32x4*)(L + L_A + ((2 * n1 + m) >> 2) * ASZ + q * 4096 + ((2 * n1 + m) & 3) * 1024 + lane * 16);
    o0[m] += f32x4{lo16(xr[m].x), hi16(xr[m].x), lo16(xr[m].y), hi16(xr[m].y)};
    o1[m] += f32x4{lo16(xr[m].z), hi16(xr[m].z), lo16(xr[m].w), hi16(xr[m].w)};
  }
  poll_flags(p.ws, WS_FP + (b * 16 + (lane & 3)) * 8, (lane & 3) != q, ep, dead);
  __builtin_amdgcn_fence(__ATOMIC_ACQUIRE, "wavefront");
  STAMP(10);
  u32x4 pa[3], pb[3];
#pragma unroll
  for (int j = 0; j < 3; ++j) {
    const int src = (q + 1 + j) & 3;
    pa[j] = __builtin_amdgcn_raw_buffer_load_b128(rw, lane * 16, WS_P + ((b * 4 + src) * 7 + n0) * 1024, 16);
    pb[j] = __builtin_amdgcn_raw_buffer_load_b128(rw, lane * 16, WS_P + ((b * 4 + src) * 7 + n1) * 1024, 16);
  }
#pragma unroll
  for (int j = 0; j < 3; ++j) {
    o0[0] += f32x4{lo16(pa[j].x), hi16(pa[j].x), lo16(pa[j].y), hi16(pa[j].y)};
    o0[1] += f32x4{lo16(pa[j].z), hi16(pa[j].z), lo16(pa[j].w), hi16(pa[j].w)};
    o1[0] += f32x4{lo16(pb[j].x), hi16(pb[j].x), lo16(pb[j].y), hi16(pb[j].y)};
    o1[1] += f32x4{lo16(pb[j].z), hi16(pb[j].z), lo16(pb[j].w), hi16(pb[j].w)};
  }
  const rsrc_t ro = mk(p.out, S_ * D_ * 2);
#pragma unroll
  for (int m = 0; m < 2; ++m) {
    const u32x2 l2 = {pk(o0[m][0], o0[m][1]), pk(o0[m][2], o0[m][3])}, h2 = {pk(o1[m][0], o1[m][1]), pk(o1[m][2], o1[m][3])};
    if (w < 3) { u32x4 o = {l2.x, l2.y, h2.x, h2.y}; __builtin_amdgcn_raw_buffer_store_b128(o, ro, xo + m * 4 * 896, 0, 0); }
    else if (c & 1) __builtin_amdgcn_raw_buffer_store_b64(h2, ro, xo + m * 4 * 896 + 8, 0, 0);
    else __builtin_amdgcn_raw_buffer_store_b64(l2, ro, xo + m * 4 * 896, 0, 0);
  }
  STAMP(11); CSTAMP(1);
}

extern "C" void kernel_launch(void* const* d_in, const int* in_sizes, int n_in, void* d_out, int out_size, void* d_ws, size_t ws_size,
                              hipStream_t stream) {
  Params p;
  p.x = (const char*)d_in[0]; p.wsr = (const char*)d_in[1]; p.wsi = (const char*)d_in[2]; p.wdr = (const char*)d_in[3];
  p.wdi = (const char*)d_in[4]; p.w1 = (const char*)d_in[6]; p.w2 = (const char*)d_in[7];
  p.out = (char*)d_out; p.ws = (char*)d_ws;
  (void)hipFuncSetAttribute((const void*)fnet_kernel, hipFuncAttributeMaxDynamicSharedMemorySize, LDS_BYTES);
  fnet_kernel<<<256, 384, LDS_BYTES, stream>>>(p);
}
```

```cpp
#include <hip/hip_runtime.h>
#include <stdint.h>

struct Params {
  const char* x; const char* wsr; const char* wsi; const char* wdr; const char* wdi; const char* w1; const char* w2;
  char* out; char* ws;
};
constexpr int LDS_BYTES = 154688;
constexpr size_t WS_OFF = (size_t)0 << 20;

__global__ __launch_bounds__(448, 2) void fnet_kernel(Params p) {
  asm volatile(
    "s_load_dwordx16 s[4:19], s[0:1], 0\n"
    "s_load_dwordx2 s[20:21], s[0:1], 0x40\n"
    "v_readfirstlane_b32 s22, v0\n"
    "v_and_b32 v0, 63, v0\n"
    "s_and_b32 s23, s2, 1\n"
    "s_bfe_u32 s24, s2, 0x20001\n"
    "s_lshr_b32 s25, s2, 5\n"
    "s_bfe_u32 s26, s2, 0x20003\n"
    "s_lshl3_add_u32 s27, s23, s25\n"
    "s_mul_i32 s28, s24, 7\n"
    "s_add_u32 s28, s28, 1\n"
    "s_lshr_b32 s28, s28, 1\n"
    "s_add_u32 s28, s28, s26\n"
    "s_lshr_b32 s22, s22, 6\n"
    "s_cmp_eq_u32 s26, 3\n"
    "s_cselect_b32 s30, 1, 2\n"
    "s_and_b32 s29, s2, 0x1a\n"
    "s_cmp_eq_u32 s29, 0x1a\n"
    "s_cselect_b32 s29, 1, 0\n"
    "s_cselect_b32 s30, 0, s30\n"
    "s_cmp_lt_u32 s22, 4\n"
    "s_cbranch_scc1 .Lcmp_%=\n"
    "s_cmp_eq_u32 s22, 6\n"
    "s_cbranch_scc0 .Lldr_%=\n"
    "s_getpc_b64 s[70:71]\n"
    ".Licp_%=:\n"
    "s_lshr_b32 s72, s2, 3\n"
    "s_and_b32 s72, s72, 31\n"
    "s_mul_i32 s72, s72, 1088\n"
    "s_min_u32 s72, s72, .Lkend_%=-.Licp_%=-1088\n"
    "s_add_u32 s70, s70, s72\n"
    "s_addc_u32 s71, s71, 0\n"
    "s_load_dword s72, s[70:71], 0\n"
    "s_load_dword s72, s[70:71], 64\n"
    "s_load_dword s72, s[70:71], 128\n"
    "s_load_dword s72, s[70:71], 192\n"
    "s_load_dword s72, s[70:71], 256\n"
    "s_load_dword s72, s[70:71], 320\n"
    "s_load_dword s72, s[70:71], 384\n"
    "s_load_dword s72, s[70:71], 448\n"
    "s_load_dword s72, s[70:71], 512\n"
    "s_load_dword s72, s[70:71], 576\n"
    "s_load_dword s72, s[70:71], 640\n"
    "s_load_dword s72, s[70:71], 704\n"
    "s_load_dword s72, s[70:71], 768\n"
    "s_load_dword s72, s[70:71], 832\n"
    "s_load_dword s72, s[70:71], 896\n"
    "s_load_dword s72, s[70:71], 960\n"
    "s_load_dword s72, s[70:71], 1024\n"
    "s_branch .Lhlp_%=\n"
    ".Lldr_%=:\n"
    "s_setprio 3\n"
    "v_lshrrev_b32 v1, 3, v0\n"
    "v_bfe_u32 v2, v0, 4, 1\n"
    "v_and_b32 v3, 7, v0\n"
    "v_xor_b32 v2, v2, v3\n"
    "v_lshlrev_b32 v2, 4, v2\n"
    "v_lshlrev_b32 v16, 4, v0\n"
    "s_sub_u32 s56, s22, 4\n"
    "s_lshl_b32 s57, s56, 13\n"
    "s_lshl_b32 s58, s56, 12\n"
    "s_mul_i32 s59, s56, 7168\n"
    "s_cmp_eq_u32 s29, 1\n"
    "s_cbranch_scc1 .Llidle_%=\n"
    "v_mul_u32_u24 v3, 896, v1\n"
    "v_add_u32 v4, v3, v2\n"
    "v_add_u32 v3, 6144, v3\n"
    "v_xor_b32 v5, 32, v2\n"
    "v_add_u32 v5, v3, v5\n"
    "v_add_u32 v3, 6144, v3\n"
    "v_xor_b32 v6, 64, v2\n"
    "v_add_u32 v6, v3, v6\n"
    "v_add_u32 v3, 6144, v3\n"
    "v_xor_b32 v7, 96, v2\n"
    "v_add_u32 v7, v3, v7\n"
    "s_lshl1_add_u32 s60, s27, s56\n"
    "s_mul_i32 s60, s60, 57344\n"
    "s_lshl1_add_u32 s62, s26, s25\n"
    "s_cmp_ge_u32 s62, 7\n"
    "s_cselect_b32 s78, 7, 0\n"
    "s_sub_u32 s62, s62, s78\n"
    "s_lshl_b32 s62, s62, 7\n"
    "s_mul_i32 s63, s62, 1792\n"
    "s_cmp_ge_u32 s28, 7\n"
    "s_cselect_b32 s78, 7, 0\n"
    "s_sub_u32 s61, s28, s78\n"
    "s_lshl1_add_u32 s61, s61, s56\n"
    "s_mul_i32 s61, s61, 28672\n"
    "s_waitcnt lgkmcnt(0)\n"
    "s_cmp_ge_u32 s28, 7\n"
    "s_cselect_b32 s40, s12, s10\n"
    "s_cselect_b32 s41, s13, s11\n"
    "s_and_b32 s41, s41, 0xffff\n"
    "s_mov_b32 s42, 401408\n"
    "s_mov_b32 s43, 0x20000\n"
    "s_mov_b32 s36, s4\n"
    "s_and_b32 s37, s5, 0xffff\n"
    "s_mov_b32 s38, 1835008\n"
    "s_mov_b32 s39, 0x20000\n"
    "s_add_u32 m0, s58, 131072\n"
    "s_add_u32 s78, s61, s62\n"
    "buffer_load_dwordx4 v4, s[40:43], s78 offen lds\n"
    "buffer_load_dwordx4 v5, s[40:43], s78 offen offset:1024 lds\n"
    "buffer_load_dwordx4 v6, s[40:43], s78 offen offset:2048 lds\n"
    "buffer_load_dwordx4 v7, s[40:43], s78 offen offset:3072 lds\n"
    "s_add_u32 m0, s57, 0\n"
    "s_add_u32 s78, s60, s62\n"
    "buffer_load_dwordx4 v4, s[36:39], s78 offen lds\n"
    "buffer_load_dwordx4 v5, s[36:39], s78 offen offset:1024 lds\n"
    "buffer_load_dwordx4 v6, s[36:39], s78 offen offset:2048 lds\n"
    "buffer_load_dwordx4 v7, s[36:39], s78 offen offset:3072 lds\n"
    "s_add_u32 m0, s57, 4096\n"
    "s_add_u32 s78, s78, 28672\n"
    "buffer_load_dwordx4 v4, s[36:39], s78 offen lds\n"
    "buffer_load_dwordx4 v5, s[36:39], s78 offen offset:1024 lds\n"
    "buffer_load_dwordx4 v6, s[36:39], s78 offen offset:2048 lds\n"
    "buffer_load_dwordx4 v7, s[36:39], s78 offen offset:3072 lds\n"
    "s_add_u32 s62, s62, 128\n"
    "s_cmp_eq_u32 s62, 896\n"
    "s_cselect_b32 s62, 0, s62\n"
    "s_add_u32 m0, s58, 139264\n"
    "s_add_u32 s78, s61, s62\n"
    "buffer_load_dwordx4 v4, s[40:43], s78 offen lds\n"
    "buffer_load_dwordx4 v5, s[40:43], s78 offen offset:1024 lds\n"
    "buffer_load_dwordx4 v6, s[40:43], s78 offen offset:2048 lds\n"
    "buffer_load_dwordx4 v7, s[40:43], s78 offen offset:3072 lds\n"
    "s_add_u32 m0, s57, 16384\n"
    "s_add_u32 s78, s60, s62\n"
    "buffer_load_dwordx4 v4, s[36:39], s78 offen lds\n"
    "buffer_load_dwordx4 v5, s[36:39], s78 offen offset:1024 lds\n"
    "buffer_load_dwordx4 v6, s[36:39], s78 offen offset:2048 lds\n"
    "buffer_load_dwordx4 v7, s[36:39], s78 offen offset:3072 lds\n"
    "s_add_u32 m0, s57, 20480\n"
    "s_add_u32 s78, s78, 28672\n"
    "buffer_load_dwordx4 v4, s[36:39], s78 offen lds\n"
    "buffer_load_dwordx4 v5, s[36:39], s78 offen offset:1024 lds\n"
    "buffer_load_dwordx4 v6, s[36:39], s78 offen offset:2048 lds\n"
    "buffer_load_dwordx4 v7, s[36:39], s78 offen offset:3072 lds\n"
    "s_add_u32 s62, s62, 128\n"
    "s_cmp_eq_u32 s62, 896\n"
    "s_cselect_b32 s62, 0, s62\n"
    "v_lshrrev_b32 v1, 3, v0\n"
    "v_mul_u32_u24 v3, 4096, v1\n"
    "v_add_u32 v8, v3, v2\n"
    "v_add_u32 v3, 31744, v3\n"
    "v_xor_b32 v9, 32, v2\n"
    "v_add_u32 v9, v3, v9\n"
    "v_add_u32 v3, 31744, v3\n"
    "v_xor_b32 v10, 64, v2\n"
    "v_add_u32 v10, v3, v10\n"
    "v_add_u32 v3, 31744, v3\n"
    "v_xor_b32 v11, 96, v2\n"
    "v_add_u32 v11, v3, v11\n"
    "v_lshrrev_b32 v1, 4, v0\n"
    "v_and_b32 v17, 15, v0\n"
    "v_lshlrev_b32 v18, 1, v1\n"
    "v_xor_b32 v17, v17, v18\n"
    "v_lshlrev_b32 v17, 4, v17\n"
    "v_mul_u32_u24 v3, 3584, v1\n"
    "v_add_u32 v12, v3, v17\n"
    "v_add_u32 v3, 13312, v3\n"
    "v_xor_b32 v13, 0, v17\n"
    "v_add_u32 v13, v3, v13\n"
    "v_add_u32 v3, 13312, v3\n"
    "v_xor_b32 v14, 128, v17\n"
    "v_add_u32 v14, v3, v14\n"
    "v_add_u32 v3, 13312, v3\n"
    "v_xor_b32 v15, 128, v17\n"
    "v_add_u32 v15, v3, v15\n"
    "s_mov_b32 s44, s14\n"
    "s_and_b32 s45, s15, 0xffff\n"
    "s_mov_b32 s46, 1605632\n"
    "s_mov_b32 s47, 0x20000\n"
    "s_mov_b32 s52, s20\n"
    "s_and_b32 s53, s21, 0xffff\n"
    "s_mov_b32 s54, 25165824\n"
    "s_mov_b32 s55, 0x20000\n"
    "s_cmp_ge_u32 s24, 2\n"
    "s_cselect_b32 s48, s8, s6\n"
    "s_cselect_b32 s49, s9, s7\n"
    "s_and_b32 s49, s49, 0xffff\n"
    "s_mov_b32 s50, 8388608\n"
    "s_mov_b32 s51, 0x20000\n"
    "s_mul_i32 s64, s56, 114688\n"
    "s_lshl_b32 s78, s28, 8\n"
    "s_add_u32 s64, s64, s78\n"
    "s_lshl1_add_u32 s65, s27, s56\n"
    "s_lshl_b32 s65, s65, 18\n"
    "s_and_b32 s78, s24, 1\n"
    "s_lshl_b32 s79, s78, 11\n"
    "s_add_u32 s65, s65, s79\n"
    "s_mul_i32 s66, s26, 256\n"
    "s_mul_i32 s68, s25, 0\n"
    "s_add_u32 s66, s66, s68\n"
    "s_lshr_b32 s68, s24, 1\n"
    "s_lshl1_add_u32 s68, s68, s23\n"
    "s_mul_i32 s68, s68, 0\n"
    "s_add_u32 s66, s66, s68\n"
    "s_and_b32 s66, s66, 1023\n"
    "s_mov_b32 s68, s66\n"
    "s_mov_b32 s80, 0\n"
    "s_mov_b32 s81, 0\n"
    "s_lshr_b32 s79, s24, 1\n"
    "s_lshl2_add_u32 s79, s79, s26\n"
    "s_lshl1_add_u32 s79, s79, s78\n"
    "s_mul_i32 s67, s79, 229376\n"
    "s_add_u32 s67, s67, s59\n"
    "s_add_u32 s67, s67, 1048576\n"
    "s_mul_i32 s78, s26, 28\n"
    "s_lshl1_add_u32 s69, s28, s78\n"
    "s_mul_i32 s69, s69, 14336\n"
    "s_add_u32 s69, s69, s59\n"
    "s_add_u32 s69, s69, 5242880\n"
    "s_mul_i32 s79, s24, 7\n"
    "s_add_u32 s74, s78, s79\n"
    "s_mul_i32 s74, s74, 14336\n"
    "s_add_u32 s74, s74, s59\n"
    "s_add_u32 s74, s74, 5242880\n"
    "s_mul_i32 s73, s27, 28\n"
    "s_add_u32 s73, s73, s79\n"
    "s_lshl_b32 s73, s73, 14\n"
    "s_add_u32 s73, s73, s57\n"
    "s_add_u32 s73, s73, 7340032\n"
    "s_lshl1_add_u32 s77, s28, s30\n"
    "s_sub_u32 s77, s77, s79\n"
    "s_sub_u32 s76, 7, s30\n"
    "s_cmp_ge_u32 s25, s76\n"
    "s_cselect_b32 s78, s76, 0\n"
    "s_sub_u32 s75, s25, s78\n"
    "s_add_u32 s70, s57, 0\n"
    "s_add_u32 s71, s59, 81920\n"
    "s_mul_i32 s78, s26, 0x11111111\n"
    "s_mul_i32 s84, s25, 0\n"
    "s_add_u32 s80, s78, 0x32132100\n"
    "s_and_b32 s80, s80, 0x33333333\n"
    "s_or_b32 s80, s80, 0x44400040\n"
    "s_add_u32 s81, s78, 0x32132100\n"
    "s_and_b32 s81, s81, 0x33333333\n"
    "s_or_b32 s81, s81, 0xccc888c8\n"
    "s_mov_b64 s[82:83], s[80:81]\n"
    "s_and_b32 s66, s80, 15\n"
    "s_lshl_b32 s66, s66, 7\n"
    "s_lshr_b64 s[80:81], s[80:81], 4\n"
    "s_and_b32 s68, s82, 15\n"
    "s_lshl_b32 s68, s68, 7\n"
    "s_lshr_b64 s[82:83], s[82:83], 4\n"
    "s_waitcnt vmcnt(12)\n"
    "s_barrier\n"
    "s_add_u32 m0, s58, 114688\n"
    "s_add_u32 s78, s61, s62\n"
    "buffer_load_dwordx4 v4, s[40:43], s78 offen lds\n"
    "buffer_load_dwordx4 v5, s[40:43], s78 offen offset:1024 lds\n"
    "buffer_load_dwordx4 v6, s[40:43], s78 offen offset:2048 lds\n"
    "buffer_load_dwordx4 v7, s[40:43], s78 offen offset:3072 lds\n"
    "s_add_u32 m0, s57, 32768\n"
    "s_add_u32 s78, s60, s62\n"
    "buffer_load_dwordx4 v4, s[36:39], s78 offen lds\n"
    "buffer_load_dwordx4 v5, s[36:39], s78 offen offset:1024 lds\n"
    "buffer_load_dwordx4 v6, s[36:39], s78 offen offset:2048 lds\n"
    "buffer_load_dwordx4 v7, s[36:39], s78 offen offset:3072 lds\n"
    "s_add_u32 m0, s57, 36864\n"
    "s_add_u32 s78, s78, 28672\n"
    "buffer_load_dwordx4 v4, s[36:39], s78 offen lds\n"
    "buffer_load_dwordx4 v5, s[36:39], s78 offen offset:1024 lds\n"
    "buffer_load_dwordx4 v6, s[36:39], s78 offen offset:2048 lds\n"
    "buffer_load_dwordx4 v7, s[36:39], s78 offen offset:3072 lds\n"
    "s_add_u32 s62, s62, 128\n"
    "s_cmp_eq_u32 s62, 896\n"
    "s_cselect_b32 s62, 0, s62\n"
    "s_waitcnt vmcnt(12)\n"
    "s_barrier\n"
    "s_add_u32 m0, s58, 122880\n"
    "s_add_u32 s78, s61, s62\n"
    "buffer_load_dwordx4 v4, s[40:43], s78 offen lds\n"
    "buffer_load_dwordx4 v5, s[40:43], s78 offen offset:1024 lds\n"
    "buffer_load_dwordx4 v6, s[40:43], s78 offen offset:2048 lds\n"
    "buffer_load_dwordx4 v7, s[40:43], s78 offen offset:3072 lds\n"
    "s_add_u32 m0, s57, 49152\n"
    "s_add_u32 s78, s60, s62\n"
    "buffer_load_dwordx4 v4, s[36:39], s78 offen lds\n"
    "buffer_load_dwordx4 v5, s[36:39], s78 offen offset:1024 lds\n"
    "buffer_load_dwordx4 v6, s[36:39], s78 offen offset:2048 lds\n"
    "buffer_load_dwordx4 v7, s[36:39], s78 offen offset:3072 lds\n"
    "s_add_u32 m0, s57, 53248\n"
    "s_add_u32 s78, s78, 28672\n"
    "buffer_load_dwordx4 v4, s[36:39], s78 offen lds\n"
    "buffer_load_dwordx4 v5, s[36:39], s78 offen offset:1024 lds\n"
    "buffer_load_dwordx4 v6, s[36:39], s78 offen offset:2048 lds\n"
    "buffer_load_dwordx4 v7, s[36:39], s78 offen offset:3072 lds\n"
    "s_add_u32 s62, s62, 128\n"
    "s_cmp_eq_u32 s62, 896\n"
    "s_cselect_b32 s62, 0, s62\n"
    "s_waitcnt vmcnt(12)\n"
    "s_barrier\n"
    "s_add_u32 m0, s58, 98304\n"
    "s_add_u32 s78, s61, s62\n"
    "buffer_load_dwordx4 v4, s[40:43], s78 offen lds\n"
    "buffer_load_dwordx4 v5, s[40:43], s78 offen offset:1024 lds\n"
    "buffer_load_dwordx4 v6, s[40:43], s78 offen offset:2048 lds\n"
    "buffer_load_dwordx4 v7, s[40:43], s78 offen offset:3072 lds\n"
    "s_add_u32 m0, s57, 65536\n"
    "s_add_u32 s78, s60, s62\n"
    "buffer_load_dwordx4 v4, s[36:39], s78 offen lds\n"
    "buffer_load_dwordx4 v5, s[36:39], s78 offen offset:1024 lds\n"
    "buffer_load_dwordx4 v6, s[36:39], s78 offen offset:2048 lds\n"
    "buffer_load_dwordx4 v7, s[36:39], s78 offen offset:3072 lds\n"
    "s_add_u32 m0, s57, 69632\n"
    "s_add_u32 s78, s78, 28672\n"
    "buffer_load_dwordx4 v4, s[36:39], s78 offen lds\n"
    "buffer_load_dwordx4 v5, s[36:39], s78 offen offset:1024 lds\n"
    "buffer_load_dwordx4 v6, s[36:39], s78 offen offset:2048 lds\n"
    "buffer_load_dwordx4 v7, s[36:39], s78 offen offset:3072 lds\n"
    "s_add_u32 s62, s62, 128\n"
    "s_cmp_eq_u32 s62, 896\n"
    "s_cselect_b32 s62, 0, s62\n"
    "s_waitcnt vmcnt(12)\n"
    "s_barrier\n"
    "s_add_u32 m0, s58, 106496\n"
    "s_add_u32 s78, s61, s62\n"
    "buffer_load_dwordx4 v4, s[40:43], s78 offen lds\n"
    "buffer_load_dwordx4 v5, s[40:43], s78 offen offset:1024 lds\n"
    "buffer_load_dwordx4 v6, s[40:43], s78 offen offset:2048 lds\n"
    "buffer_load_dwordx4 v7, s[40:43], s78 offen offset:3072 lds\n"
    "s_add_u32 m0, s57, 0\n"
    "s_add_u32 s78, s60, s62\n"
    "buffer_load_dwordx4 v4, s[36:39], s78 offen lds\n"
    "buffer_load_dwordx4 v5, s[36:39], s78 offen offset:1024 lds\n"
    "buffer_load_dwordx4 v6, s[36:39], s78 offen offset:2048 lds\n"
    "buffer_load_dwordx4 v7, s[36:39], s78 offen offset:3072 lds\n"
    "s_add_u32 m0, s57, 4096\n"
    "s_add_u32 s78, s78, 28672\n"
    "buffer_load_dwordx4 v4, s[36:39], s78 offen lds\n"
    "buffer_load_dwordx4 v5, s[36:39], s78 offen offset:1024 lds\n"
    "buffer_load_dwordx4 v6, s[36:39], s78 offen offset:2048 lds\n"
    "buffer_load_dwordx4 v7, s[36:39], s78 offen offset:3072 lds\n"
    "s_add_u32 s62, s62, 128\n"
    "s_cmp_eq_u32 s62, 896\n"
    "s_cselect_b32 s62, 0, s62\n"
    "s_waitcnt vmcnt(12)\n"
    "s_barrier\n"
    "s_add_u32 m0, s58, 81920\n"
    "s_add_u32 s78, s61, s62\n"
    "buffer_load_dwordx4 v4, s[40:43], s78 offen lds\n"
    "buffer_load_dwordx4 v5, s[40:43], s78 offen offset:1024 lds\n"
    "buffer_load_dwordx4 v6, s[40:43], s78 offen offset:2048 lds\n"
    "buffer_load_dwordx4 v7, s[40:43], s78 offen offset:3072 lds\n"
    "s_add_u32 m0, s57, 16384\n"
    "s_add_u32 s78, s60, s62\n"
    "buffer_load_dwordx4 v4, s[36:39], s78 offen lds\n"
    "buffer_load_dwordx4 v5, s[36:39], s78 offen offset:1024 lds\n"
    "buffer_load_dwordx4 v6, s[36:39], s78 offen offset:2048 lds\n"
    "buffer_load_dwordx4 v7, s[36:39], s78 offen offset:3072 lds\n"
    "s_add_u32 m0, s57, 20480\n"
    "s_add_u32 s78, s78, 28672\n"
    "buffer_load_dwordx4 v4, s[36:39], s78 offen lds\n"
    "buffer_load_dwordx4 v5, s[36:39], s78 offen offset:1024 lds\n"
    "buffer_load_dwordx4 v6, s[36:39], s78 offen offset:2048 lds\n"
    "buffer_load_dwordx4 v7, s[36:39], s78 offen offset:3072 lds\n"
    "s_add_u32 s62, s62, 128\n"
    "s_cmp_eq_u32 s62, 896\n"
    "s_cselect_b32 s62, 0, s62\n"
    "s_add_u32 m0, s57, 32768\n"
    "s_add_u32 s78, s64, s63\n"
    "buffer_load_dwordx4 v12, s[44:47], s78 offen lds\n"
    "buffer_load_dwordx4 v13, s[44:47], s78 offen offset:1024 lds\n"
    "buffer_load_dwordx4 v14, s[44:47], s78 offen offset:2048 lds\n"
    "buffer_load_dwordx4 v15, s[44:47], s78 offen offset:3072 lds\n"
    "s_add_u32 m0, s57, 36864\n"
    "s_add_u32 s78, s78, 57344\n"
    "buffer_load_dwordx4 v12, s[44:47], s78 offen lds\n"
    "buffer_load_dwordx4 v13, s[44:47], s78 offen offset:1024 lds\n"
    "buffer_load_dwordx4 v14, s[44:47], s78 offen offset:2048 lds\n"
    "buffer_load_dwordx4 v15, s[44:47], s78 offen offset:3072 lds\n"
    "s_add_u32 s63, s63, 229376\n"
    "s_cmp_eq_u32 s63, 1605632\n"
    "s_cselect_b32 s63, 0, s63\n"
    "s_waitcnt vmcnt(20)\n"
    "s_barrier\n"
    "s_add_u32 m0, s57, 49152\n"
    "s_add_u32 s78, s64, s63\n"
    "buffer_load_dwordx4 v12, s[44:47], s78 offen lds\n"
    "buffer_load_dwordx4 v13, s[44:47], s78 offen offset:1024 lds\n"
    "buffer_load_dwordx4 v14, s[44:47], s78 offen offset:2048 lds\n"
    "buffer_load_dwordx4 v15, s[44:47], s78 offen offset:3072 lds\n"
    "s_add_u32 m0, s57, 53248\n"
    "s_add_u32 s78, s78, 57344\n"
    "buffer_load_dwordx4 v12, s[44:47], s78 offen lds\n"
    "buffer_load_dwordx4 v13, s[44:47], s78 offen offset:1024 lds\n"
    "buffer_load_dwordx4 v14, s[44:47], s78 offen offset:2048 lds\n"
    "buffer_load_dwordx4 v15, s[44:47], s78 offen offset:3072 lds\n"
    "s_add_u32 s63, s63, 229376\n"
    "s_cmp_eq_u32 s63, 1605632\n"
    "s_cselect_b32 s63, 0, s63\n"
    "s_waitcnt vmcnt(16)\n"
    "s_barrier\n"
    "s_add_u32 m0, s57, 65536\n"
    "s_add_u32 s78, s64, s63\n"
    "buffer_load_dwordx4 v12, s[44:47], s78 offen lds\n"
    "buffer_load_dwordx4 v13, s[44:47], s78 offen offset:1024 lds\n"
    "buffer_load_dwordx4 v14, s[44:47], s78 offen offset:2048 lds\n"
    "buffer_load_dwordx4 v15, s[44:47], s78 offen offset:3072 lds\n"
    "s_add_u32 m0, s57, 69632\n"
    "s_add_u32 s78, s78, 57344\n"
    "buffer_load_dwordx4 v12, s[44:47], s78 offen lds\n"
    "buffer_load_dwordx4 v13, s[44:47], s78 offen offset:1024 lds\n"
    "buffer_load_dwordx4 v14, s[44:47], s78 offen offset:2048 lds\n"
    "buffer_load_dwordx4 v15, s[44:47], s78 offen offset:3072 lds\n"
    "s_add_u32 s63, s63, 229376\n"
    "s_cmp_eq_u32 s63, 1605632\n"
    "s_cselect_b32 s63, 0, s63\n"
    "s_add_u32 m0, s57, 131072\n"
    "s_add_u32 s78, s64, s63\n"
    "buffer_load_dwordx4 v12, s[44:47], s78 offen lds\n"
    "buffer_load_dwordx4 v13, s[44:47], s78 offen offset:1024 lds\n"
    "buffer_load_dwordx4 v14, s[44:47], s78 offen offset:2048 lds\n"
    "buffer_load_dwordx4 v15, s[44:47], s78 offen offset:3072 lds\n"
    "s_add_u32 m0, s57, 135168\n"
    "s_add_u32 s78, s78, 57344\n"
    "buffer_load_dwordx4 v12, s[44:47], s78 offen lds\n"
    "buffer_load_dwordx4 v13, s[44:47], s78 offen offset:1024 lds\n"
    "buffer_load_dwordx4 v14, s[44:47], s78 offen offset:2048 lds\n"
    "buffer_load_dwordx4 v15, s[44:47], s78 offen offset:3072 lds\n"
    "s_add_u32 s63, s63, 229376\n"
    "s_cmp_eq_u32 s63, 1605632\n"
    "s_cselect_b32 s63, 0, s63\n"
    "s_waitcnt vmcnt(24)\n"
    "s_barrier\n"
    "s_add_u32 m0, s57, 114688\n"
    "s_add_u32 s78, s64, s63\n"
    "buffer_load_dwordx4 v12, s[44:47], s78 offen lds\n"
    "buffer_load_dwordx4 v13, s[44:47], s78 offen offset:1024 lds\n"
    "buffer_load_dwordx4 v14, s[44:47], s78 offen offset:2048 lds\n"
    "buffer_load_dwordx4 v15, s[44:47], s78 offen offset:3072 lds\n"
    "s_add_u32 m0, s57, 118784\n"
    "s_add_u32 s78, s78, 57344\n"
    "buffer_load_dwordx4 v12, s[44:47], s78 offen lds\n"
    "buffer_load_dwordx4 v13, s[44:47], s78 offen offset:1024 lds\n"
    "buffer_load_dwordx4 v14, s[44:47], s78 offen offset:2048 lds\n"
    "buffer_load_dwordx4 v15, s[44:47], s78 offen offset:3072 lds\n"
    "s_add_u32 s63, s63, 229376\n"
    "s_cmp_eq_u32 s63, 1605632\n"
    "s_cselect_b32 s63, 0, s63\n"
    "s_waitcnt vmcnt(24)\n"
    "s_barrier\n"
    "s_add_u32 m0, s57, 98304\n"
    "s_add_u32 s78, s64, s63\n"
    "buffer_load_dwordx4 v12, s[44:47], s78 offen lds\n"
    "buffer_load_dwordx4 v13, s[44:47], s78 offen offset:1024 lds\n"
    "buffer_load_dwordx4 v14, s[44:47], s78 offen offset:2048 lds\n"
    "buffer_load_dwordx4 v15, s[44:47], s78 offen offset:3072 lds\n"
    "s_add_u32 m0, s57, 102400\n"
    "s_add_u32 s78, s78, 57344\n"
    "buffer_load_dwordx4 v12, s[44:47], s78 offen lds\n"
    "buffer_load_dwordx4 v13, s[44:47], s78 offen offset:1024 lds\n"
    "buffer_load_dwordx4 v14, s[44:47], s78 offen offset:2048 lds\n"
    "buffer_load_dwordx4 v15, s[44:47], s78 offen offset:3072 lds\n"
    "s_add_u32 s63, s63, 229376\n"
    "s_cmp_eq_u32 s63, 1605632\n"
    "s_cselect_b32 s63, 0, s63\n"
    "s_add_u32 m0, s57, 81920\n"
    "s_add_u32 s78, s64, s63\n"
    "buffer_load_dwordx4 v12, s[44:47], s78 offen lds\n"
    "buffer_load_dwordx4 v13, s[44:47], s78 offen offset:1024 lds\n"
    "buffer_load_dwordx4 v14, s[44:47], s78 offen offset:2048 lds\n"
    "buffer_load_dwordx4 v15, s[44:47], s78 offen offset:3072 lds\n"
    "s_add_u32 m0, s57, 86016\n"
    "s_add_u32 s78, s78, 57344\n"
    "buffer_load_dwordx4 v12, s[44:47], s78 offen lds\n"
    "buffer_load_dwordx4 v13, s[44:47], s78 offen offset:1024 lds\n"
    "buffer_load_dwordx4 v14, s[44:47], s78 offen offset:2048 lds\n"
    "buffer_load_dwordx4 v15, s[44:47], s78 offen offset:3072 lds\n"
    "s_add_u32 s63, s63, 229376\n"
    "s_cmp_eq_u32 s63, 1605632\n"
    "s_cselect_b32 s63, 0, s63\n"
    "s_waitcnt vmcnt(32)\n"
    "s_barrier\n"
    "s_add_u32 m0, s57, 0\n"
    "s_add_u32 s78, s65, s66\n"
    "buffer_load_dwordx4 v8, s[48:51], s78 offen lds\n"
    "buffer_load_dwordx4 v9, s[48:51], s78 offen offset:1024 lds\n"
    "buffer_load_dwordx4 v10, s[48:51], s78 offen offset:2048 lds\n"
    "buffer_load_dwordx4 v11, s[48:51], s78 offen offset:3072 lds\n"
    "s_add_u32 m0, s57, 4096\n"
    "s_add_u32 s78, s78, 131072\n"
    "buffer_load_dwordx4 v8, s[48:51], s78 offen lds\n"
    "buffer_load_dwordx4 v9, s[48:51], s78 offen offset:1024 lds\n"
    "buffer_load_dwordx4 v10, s[48:51], s78 offen offset:2048 lds\n"
    "buffer_load_dwordx4 v11, s[48:51], s78 offen offset:3072 lds\n"
    "s_and_b32 s66, s80, 15\n"
    "s_lshl_b32 s66, s66, 7\n"
    "s_lshr_b64 s[80:81], s[80:81], 4\n"
    "s_add_u32 m0, s57, 16384\n"
    "s_add_u32 s78, s65, s66\n"
    "buffer_load_dwordx4 v8, s[48:51], s78 offen lds\n"
    "buffer_load_dwordx4 v9, s[48:51], s78 offen offset:1024 lds\n"
    "buffer_load_dwordx4 v10, s[48:51], s78 offen offset:2048 lds\n"
    "buffer_load_dwordx4 v11, s[48:51], s78 offen offset:3072 lds\n"
    "s_add_u32 m0, s57, 20480\n"
    "s_add_u32 s78, s78, 131072\n"
    "buffer_load_dwordx4 v8, s[48:51], s78 offen lds\n"
    "buffer_load_dwordx4 v9, s[48:51], s78 offen offset:1024 lds\n"
    "buffer_load_dwordx4 v10, s[48:51], s78 offen offset:2048 lds\n"
    "buffer_load_dwordx4 v11, s[48:51], s78 offen offset:3072 lds\n"
    "s_and_b32 s66, s80, 15\n"
    "s_lshl_b32 s66, s66, 7\n"
    "s_lshr_b64 s[80:81], s[80:81], 4\n"
    "s_waitcnt vmcnt(40)\n"
    "s_barrier\n"
    "s_waitcnt vmcnt(32)\n"
    "s_barrier\n"
    "s_mov_b64 s[36:37], s[52:53]\n"
    "s_cmp_eq_u32 s30, 1\n"
    "s_cselect_b32 s38, 0, s54\n"
    "s_add_u32 s78, s69, 14336\n"
    "s_add_u32 m0, s59, 139264\n"
    "s_nop 0\n"
    "buffer_load_dwordx4 v16, s[36:39], s78 offen sc1 lds\n"
    "buffer_load_dwordx4 v16, s[36:39], s78 offen offset:1024 sc1 lds\n"
    "buffer_load_dwordx4 v16, s[36:39], s78 offen offset:2048 sc1 lds\n"
    "buffer_load_dwordx4 v16, s[36:39], s78 offen offset:3072 sc1 lds\n"
    "s_add_u32 m0, s59, 143360\n"
    "s_add_u32 s78, s78, 4096\n"
    "buffer_load_dwordx4 v16, s[36:39], s78 offen sc1 lds\n"
    "buffer_load_dwordx4 v16, s[36:39], s78 offen offset:1024 sc1 lds\n"
    "buffer_load_dwordx4 v16, s[36:39], s78 offen offset:2048 sc1 lds\n"
    "s_add_u32 m0, s57, 32768\n"
    "s_add_u32 s78, s65, s66\n"
    "buffer_load_dwordx4 v8, s[48:51], s78 offen lds\n"
    "buffer_load_dwordx4 v9, s[48:51], s78 offen offset:1024 lds\n"
    "buffer_load_dwordx4 v10, s[48:51], s78 offen offset:2048 lds\n"
    "buffer_load_dwordx4 v11, s[48:51], s78 offen offset:3072 lds\n"
    "s_add_u32 m0, s57, 36864\n"
    "s_add_u32 s78, s78, 131072\n"
    "buffer_load_dwordx4 v8, s[48:51], s78 offen lds\n"
    "buffer_load_dwordx4 v9, s[48:51], s78 offen offset:1024 lds\n"
    "buffer_load_dwordx4 v10, s[48:51], s78 offen offset:2048 lds\n"
    "buffer_load_dwordx4 v11, s[48:51], s78 offen offset:3072 lds\n"
    "s_and_b32 s66, s80, 15\n"
    "s_lshl_b32 s66, s66, 7\n"
    "s_lshr_b64 s[80:81], s[80:81], 4\n"
    "s_waitcnt vmcnt(39)\n"
    "s_barrier\n"
    "s_add_u32 s78, s69, 0\n"
    "s_add_u32 m0, s59, 124928\n"
    "s_nop 0\n"
    "buffer_load_dwordx4 v16, s[52:55], s78 offen sc1 lds\n"
    "buffer_load_dwordx4 v16, s[52:55], s78 offen offset:1024 sc1 lds\n"
    "buffer_load_dwordx4 v16, s[52:55], s78 offen offset:2048 sc1 lds\n"
    "buffer_load_dwordx4 v16, s[52:55], s78 offen offset:3072 sc1 lds\n"
    "s_add_u32 m0, s59, 129024\n"
    "s_add_u32 s78, s78, 4096\n"
    "buffer_load_dwordx4 v16, s[52:55], s78 offen sc1 lds\n"
    "buffer_load_dwordx4 v16, s[52:55], s78 offen offset:1024 sc1 lds\n"
    "buffer_load_dwordx4 v16, s[52:55], s78 offen offset:2048 sc1 lds\n"
    "s_add_u32 m0, s57, 49152\n"
    "s_add_u32 s78, s65, s66\n"
    "buffer_load_dwordx4 v8, s[48:51], s78 offen lds\n"
    "buffer_load_dwordx4 v9, s[48:51], s78 offen offset:1024 lds\n"
    "buffer_load_dwordx4 v10, s[48:51], s78 offen offset:2048 lds\n"
    "buffer_load_dwordx4 v11, s[48:51], s78 offen offset:3072 lds\n"
    "s_add_u32 m0, s57, 53248\n"
    "s_add_u32 s78, s78, 131072\n"
    "buffer_load_dwordx4 v8, s[48:51], s78 offen lds\n"
    "buffer_load_dwordx4 v9, s[48:51], s78 offen offset:1024 lds\n"
    "buffer_load_dwordx4 v10, s[48:51], s78 offen offset:2048 lds\n"
    "buffer_load_dwordx4 v11, s[48:51], s78 offen offset:3072 lds\n"
    "s_and_b32 s66, s80, 15\n"
    "s_lshl_b32 s66, s66, 7\n"
    "s_lshr_b64 s[80:81], s[80:81], 4\n"
    "s_waitcnt vmcnt(46)\n"
    "s_barrier\n"
    "s_add_u32 m0, s57, 65536\n"
    "s_add_u32 s78, s65, s66\n"
    "buffer_load_dwordx4 v8, s[48:51], s78 offen lds\n"
    "buffer_load_dwordx4 v9, s[48:51], s78 offen offset:1024 lds\n"
    "buffer_load_dwordx4 v10, s[48:51], s78 offen offset:2048 lds\n"
    "buffer_load_dwordx4 v11, s[48:51], s78 offen offset:3072 lds\n"
    "s_add_u32 m0, s57, 69632\n"
    "s_add_u32 s78, s78, 131072\n"
    "buffer_load_dwordx4 v8, s[48:51], s78 offen lds\n"
    "buffer_load_dwordx4 v9, s[48:51], s78 offen offset:1024 lds\n"
    "buffer_load_dwordx4 v10, s[48:51], s78 offen offset:2048 lds\n"
    "buffer_load_dwordx4 v11, s[48:51], s78 offen offset:3072 lds\n"
    "s_and_b32 s66, s80, 15\n"
    "s_lshl_b32 s66, s66, 7\n"
    "s_lshr_b64 s[80:81], s[80:81], 4\n"
    "s_barrier\n"
    ".Lljoin_%=:\n"
    "s_mul_i32 s78, s68, 112\n"
    "s_and_b32 s68, s82, 15\n"
    "s_lshl_b32 s68, s68, 7\n"
    "s_lshr_b64 s[82:83], s[82:83], 4\n"
    "s_add_u32 s78, s78, s67\n"
    "s_add_u32 m0, s59, 81920\n"
    "s_nop 0\n"
    "buffer_load_dwordx4 v16, s[52:55], s78 offen sc1 lds\n"
    "buffer_load_dwordx4 v16, s[52:55], s78 offen offset:1024 sc1 lds\n"
    "buffer_load_dwordx4 v16, s[52:55], s78 offen offset:2048 sc1 lds\n"
    "buffer_load_dwordx4 v16, s[52:55], s78 offen offset:3072 sc1 lds\n"
    "s_add_u32 m0, s59, 86016\n"
    "s_add_u32 s78, s78, 4096\n"
    "buffer_load_dwordx4 v16, s[52:55], s78 offen sc1 lds\n"
    "buffer_load_dwordx4 v16, s[52:55], s78 offen offset:1024 sc1 lds\n"
    "buffer_load_dwordx4 v16, s[52:55], s78 offen offset:2048 sc1 lds\n"
    "s_mul_i32 s78, s68, 112\n"
    "s_and_b32 s68, s82, 15\n"
    "s_lshl_b32 s68, s68, 7\n"
    "s_lshr_b64 s[82:83], s[82:83], 4\n"
    "s_add_u32 s78, s78, s67\n"
    "s_add_u32 m0, s59, 96256\n"
    "s_nop 0\n"
    "buffer_load_dwordx4 v16, s[52:55], s78 offen sc1 lds\n"
    "buffer_load_dwordx4 v16, s[52:55], s78 offen offset:1024 sc1 lds\n"
    "s_waitcnt vmcnt(59)\n"
    "buffer_load_dwordx4 v16, s[52:55], s78 offen offset:2048 sc1 lds\n"
    "buffer_load_dwordx4 v16, s[52:55], s78 offen offset:3072 sc1 lds\n"
    "s_add_u32 m0, s59, 100352\n"
    "s_add_u32 s78, s78, 4096\n"
    "buffer_load_dwordx4 v16, s[52:55], s78 offen sc1 lds\n"
    "buffer_load_dwordx4 v16, s[52:55], s78 offen offset:1024 sc1 lds\n"
    "s_waitcnt vmcnt(59)\n"
    "buffer_load_dwordx4 v16, s[52:55], s78 offen offset:2048 sc1 lds\n"
    "s_mul_i32 s78, s68, 112\n"
    "s_and_b32 s68, s82, 15\n"
    "s_lshl_b32 s68, s68, 7\n"
    "s_lshr_b64 s[82:83], s[82:83], 4\n"
    "s_add_u32 s78, s78, s67\n"
    "s_add_u32 m0, s59, 110592\n"
    "s_nop 0\n"
    "buffer_load_dwordx4 v16, s[52:55], s78 offen sc1 lds\n"
    "buffer_load_dwordx4 v16, s[52:55], s78 offen offset:1024 sc1 lds\n"
    "buffer_load_dwordx4 v16, s[52:55], s78 offen offset:2048 sc1 lds\n"
    "s_waitcnt vmcnt(59)\n"
    "buffer_load_dwordx4 v16, s[52:55], s78 offen offset:3072 sc1 lds\n"
    "s_add_u32 m0, s59, 114688\n"
    "s_add_u32 s78, s78, 4096\n"
    "buffer_load_dwordx4 v16, s[52:55], s78 offen sc1 lds\n"
    "buffer_load_dwordx4 v16, s[52:55], s78 offen offset:1024 sc1 lds\n"
    "buffer_load_dwordx4 v16, s[52:55], s78 offen offset:2048 sc1 lds\n"
    "s_waitcnt vmcnt(37)\n"
    "s_barrier\n"
    "s_waitcnt vmcnt(14)\n"
    "s_barrier\n"
    "s_mul_i32 s78, s68, 112\n"
    "s_and_b32 s68, s82, 15\n"
    "s_lshl_b32 s68, s68, 7\n"
    "s_lshr_b64 s[82:83], s[82:83], 4\n"
    "s_add_u32 s78, s78, s67\n"
    "s_add_u32 m0, s59, 124928\n"
    "s_nop 0\n"
    "buffer_load_dwordx4 v16, s[52:55], s78 offen sc1 lds\n"
    "buffer_load_dwordx4 v16, s[52:55], s78 offen offset:1024 sc1 lds\n"
    "buffer_load_dwordx4 v16, s[52:55], s78 offen offset:2048 sc1 lds\n"
    "buffer_load_dwordx4 v16, s[52:55], s78 offen offset:3072 sc1 lds\n"
    "s_add_u32 m0, s59, 129024\n"
    "s_add_u32 s78, s78, 4096\n"
    "buffer_load_dwordx4 v16, s[52:55], s78 offen sc1 lds\n"
    "buffer_load_dwordx4 v16, s[52:55], s78 offen offset:1024 sc1 lds\n"
    "buffer_load_dwordx4 v16, s[52:55], s78 offen offset:2048 sc1 lds\n"
    "s_mul_i32 s78, s68, 112\n"
    "s_and_b32 s68, s82, 15\n"
    "s_lshl_b32 s68, s68, 7\n"
    "s_lshr_b64 s[82:83], s[82:83], 4\n"
    "s_add_u32 s78, s78, s67\n"
    "s_add_u32 m0, s59, 139264\n"
    "s_nop 0\n"
    "buffer_load_dwordx4 v16, s[52:55], s78 offen sc1 lds\n"
    "buffer_load_dwordx4 v16, s[52:55], s78 offen offset:1024 sc1 lds\n"
    "buffer_load_dwordx4 v16, s[52:55], s78 offen offset:2048 sc1 lds\n"
    "buffer_load_dwordx4 v16, s[52:55], s78 offen offset:3072 sc1 lds\n"
    "s_add_u32 m0, s59, 143360\n"
    "s_add_u32 s78, s78, 4096\n"
    "buffer_load_dwordx4 v16, s[52:55], s78 offen sc1 lds\n"
    "buffer_load_dwordx4 v16, s[52:55], s78 offen offset:1024 sc1 lds\n"
    "buffer_load_dwordx4 v16, s[52:55], s78 offen offset:2048 sc1 lds\n"
    "s_waitcnt vmcnt(21)\n"
    "s_barrier\n"
    "s_mov_b32 m0, s70\n"
    "s_add_u32 s78, s65, s66\n"
    "buffer_load_dwordx4 v8, s[48:51], s78 offen lds\n"
    "buffer_load_dwordx4 v9, s[48:51], s78 offen offset:1024 lds\n"
    "buffer_load_dwordx4 v10, s[48:51], s78 offen offset:2048 lds\n"
    "buffer_load_dwordx4 v11, s[48:51], s78 offen offset:3072 lds\n"
    "s_add_u32 m0, s70, 4096\n"
    "s_add_u32 s78, s78, 131072\n"
    "buffer_load_dwordx4 v8, s[48:51], s78 offen lds\n"
    "buffer_load_dwordx4 v9, s[48:51], s78 offen offset:1024 lds\n"
    "buffer_load_dwordx4 v10, s[48:51], s78 offen offset:2048 lds\n"
    "buffer_load_dwordx4 v11, s[48:51], s78 offen offset:3072 lds\n"
    "s_and_b32 s66, s80, 15\n"
    "s_lshl_b32 s66, s66, 7\n"
    "s_lshr_b64 s[80:81], s[80:81], 4\n"
    "s_mul_i32 s78, s68, 112\n"
    "s_and_b32 s68, s82, 15\n"
    "s_lshl_b32 s68, s68, 7\n"
    "s_lshr_b64 s[82:83], s[82:83], 4\n"
    "s_add_u32 s78, s78, s67\n"
    "s_mov_b32 m0, s71\n"
    "s_nop 0\n"
    "buffer_load_dwordx4 v16, s[52:55], s78 offen sc1 lds\n"
    "buffer_load_dwordx4 v16, s[52:55], s78 offen offset:1024 sc1 lds\n"
    "buffer_load_dwordx4 v16, s[52:55], s78 offen offset:2048 sc1 lds\n"
    "buffer_load_dwordx4 v16, s[52:55], s78 offen offset:3072 sc1 lds\n"
    "s_add_u32 m0, s71, 4096\n"
    "s_add_u32 s78, s78, 4096\n"
    "buffer_load_dwordx4 v16, s[52:55], s78 offen sc1 lds\n"
    "buffer_load_dwordx4 v16, s[52:55], s78 offen offset:1024 sc1 lds\n"
    "buffer_load_dwordx4 v16, s[52:55], s78 offen offset:2048 sc1 lds\n"
    "s_add_u32 s70, s70, 16384\n"
    "s_add_u32 s71, s71, 14336\n"
    "s_add_u32 s79, s59, 81920\n"
    "s_cmp_ge_u32 s70, 81920\n"
    "s_cselect_b32 s70, s57, s70\n"
    "s_cselect_b32 s71, s79, s71\n"
    "s_waitcnt vmcnt(29)\n"
    "s_barrier\n"
    "s_mov_b32 m0, s70\n"
    "s_add_u32 s78, s65, s66\n"
    "buffer_load_dwordx4 v8, s[48:51], s78 offen lds\n"
    "buffer_load_dwordx4 v9, s[48:51], s78 offen offset:1024 lds\n"
    "buffer_load_dwordx4 v10, s[48:51], s78 offen offset:2048 lds\n"
    "buffer_load_dwordx4 v11, s[48:51], s78 offen offset:3072 lds\n"
    "s_add_u32 m0, s70, 4096\n"
    "s_add_u32 s78, s78, 131072\n"
    "buffer_load_dwordx4 v8, s[48:51], s78 offen lds\n"
    "buffer_load_dwordx4 v9, s[48:51], s78 offen offset:1024 lds\n"
    "buffer_load_dwordx4 v10, s[48:51], s78 offen offset:2048 lds\n"
    "buffer_load_dwordx4 v11, s[48:51], s78 offen offset:3072 lds\n"
    "s_and_b32 s66, s80, 15\n"
    "s_lshl_b32 s66, s66, 7\n"
    "s_lshr_b64 s[80:81], s[80:81], 4\n"
    "s_mul_i32 s78, s68, 112\n"
    "s_and_b32 s68, s82, 15\n"
    "s_lshl_b32 s68, s68, 7\n"
    "s_lshr_b64 s[82:83], s[82:83], 4\n"
    "s_add_u32 s78, s78, s67\n"
    "s_mov_b32 m0, s71\n"
    "s_nop 0\n"
    "buffer_load_dwordx4 v16, s[52:55], s78 offen sc1 lds\n"
    "buffer_load_dwordx4 v16, s[52:55], s78 offen offset:1024 sc1 lds\n"
    "buffer_load_dwordx4 v16, s[52:55], s78 offen offset:2048 sc1 lds\n"
    "buffer_load_dwordx4 v16, s[52:55], s78 offen offset:3072 sc1 lds\n"
    "s_add_u32 m0, s71, 4096\n"
    "s_add_u32 s78, s78, 4096\n"
    "buffer_load_dwordx4 v16, s[52:55], s78 offen sc1 lds\n"
    "buffer_load_dwordx4 v16, s[52:55], s78 offen offset:1024 sc1 lds\n"
    "buffer_load_dwordx4 v16, s[52:55], s78 offen offset:2048 sc1 lds\n"
    "s_add_u32 s70, s70, 16384\n"
    "s_add_u32 s71, s71, 14336\n"
    "s_add_u32 s79, s59, 81920\n"
    "s_cmp_ge_u32 s70, 81920\n"
    "s_cselect_b32 s70, s57, s70\n"
    "s_cselect_b32 s71, s79, s71\n"
    "s_waitcnt vmcnt(37)\n"
    "s_barrier\n"
    "s_mov_b32 s72, 8\n"
    ".Lld_%=:\n"
    "s_mov_b32 m0, s70\n"
    "s_add_u32 s78, s65, s66\n"
    "buffer_load_dwordx4 v8, s[48:51], s78 offen lds\n"
    "buffer_load_dwordx4 v9, s[48:51], s78 offen offset:1024 lds\n"
    "buffer_load_dwordx4 v10, s[48:51], s78 offen offset:2048 lds\n"
    "buffer_load_dwordx4 v11, s[48:51], s78 offen offset:3072 lds\n"
    "s_add_u32 m0, s70, 4096\n"
    "s_add_u32 s78, s78, 131072\n"
    "buffer_load_dwordx4 v8, s[48:51], s78 offen lds\n"
    "buffer_load_dwordx4 v9, s[48:51], s78 offen offset:1024 lds\n"
    "buffer_load_dwordx4 v10, s[48:51], s78 offen offset:2048 lds\n"
    "buffer_load_dwordx4 v11, s[48:51], s78 offen offset:3072 lds\n"
    "s_and_b32 s66, s80, 15\n"
    "s_lshl_b32 s66, s66, 7\n"
    "s_lshr_b64 s[80:81], s[80:81], 4\n"
    "s_mul_i32 s78, s68, 112\n"
    "s_and_b32 s68, s82, 15\n"
    "s_lshl_b32 s68, s68, 7\n"
    "s_lshr_b64 s[82:83], s[82:83], 4\n"
    "s_add_u32 s78, s78, s67\n"
    "s_mov_b32 m0, s71\n"
    "s_nop 0\n"
    "buffer_load_dwordx4 v16, s[52:55], s78 offen sc1 lds\n"
    "buffer_load_dwordx4 v16, s[52:55], s78 offen offset:1024 sc1 lds\n"
    "buffer_load_dwordx4 v16, s[52:55], s78 offen offset:2048 sc1 lds\n"
    "buffer_load_dwordx4 v16, s[52:55], s78 offen offset:3072 sc1 lds\n"
    "s_add_u32 m0, s71, 4096\n"
    "s_add_u32 s78, s78, 4096\n"
    "buffer_load_dwordx4 v16, s[52:55], s78 offen sc1 lds\n"
    "buffer_load_dwordx4 v16, s[52:55], s78 offen offset:1024 sc1 lds\n"
    "buffer_load_dwordx4 v16, s[52:55], s78 offen offset:2048 sc1 lds\n"
    "s_add_u32 s70, s70, 16384\n"
    "s_add_u32 s71, s71, 14336\n"
    "s_add_u32 s79, s59, 81920\n"
    "s_cmp_ge_u32 s70, 81920\n"
    "s_cselect_b32 s70, s57, s70\n"
    "s_cselect_b32 s71, s79, s71\n"
    "s_waitcnt vmcnt(45)\n"
    "s_barrier\n"
    "s_sub_u32 s72, s72, 1\n"
    "s_cbranch_scc0 .Lld_%=\n"
    "s_sub_u32 s72, 6, s30\n"
    ".Llf_%=:\n"
    "s_add_u32 s79, s77, s75\n"
    "s_cmp_ge_u32 s79, 7\n"
    "s_cselect_b32 s78, 7, 0\n"
    "s_sub_u32 s79, s79, s78\n"
    "s_lshl_b32 s78, s79, 14\n"
    "s_mov_b32 m0, s70\n"
    "s_add_u32 s78, s78, s73\n"
    "buffer_load_dwordx4 v16, s[52:55], s78 offen sc1 lds\n"
    "buffer_load_dwordx4 v16, s[52:55], s78 offen offset:1024 sc1 lds\n"
    "buffer_load_dwordx4 v16, s[52:55], s78 offen offset:2048 sc1 lds\n"
    "buffer_load_dwordx4 v16, s[52:55], s78 offen offset:3072 sc1 lds\n"
    "s_add_u32 m0, s70, 4096\n"
    "s_add_u32 s78, s78, 4096\n"
    "buffer_load_dwordx4 v16, s[52:55], s78 offen sc1 lds\n"
    "buffer_load_dwordx4 v16, s[52:55], s78 offen offset:1024 sc1 lds\n"
    "buffer_load_dwordx4 v16, s[52:55], s78 offen offset:2048 sc1 lds\n"
    "buffer_load_dwordx4 v16, s[52:55], s78 offen offset:3072 sc1 lds\n"
    "s_mul_i32 s78, s79, 14336\n"
    "s_add_u32 s78, s78, s74\n"
    "s_mov_b32 m0, s71\n"
    "s_nop 0\n"
    "buffer_load_dwordx4 v16, s[52:55], s78 offen sc1 lds\n"
    "buffer_load_dwordx4 v16, s[52:55], s78 offen offset:1024 sc1 lds\n"
    "buffer_load_dwordx4 v16, s[52:55], s78 offen offset:2048 sc1 lds\n"
    "buffer_load_dwordx4 v16, s[52:55], s78 offen offset:3072 sc1 lds\n"
    "s_add_u32 m0, s71, 4096\n"
    "s_add_u32 s78, s78, 4096\n"
    "buffer_load_dwordx4 v16, s[52:55], s78 offen sc1 lds\n"
    "buffer_load_dwordx4 v16, s[52:55], s78 offen offset:1024 sc1 lds\n"
    "buffer_load_dwordx4 v16, s[52:55], s78 offen offset:2048 sc1 lds\n"
    "s_add_u32 s75, s75, 1\n"
    "s_cmp_eq_u32 s75, s76\n"
    "s_cselect_b32 s75, 0, s75\n"
    "s_add_u32 s70, s70, 16384\n"
    "s_add_u32 s71, s71, 14336\n"
    "s_add_u32 s79, s59, 81920\n"
    "s_cmp_ge_u32 s70, 81920\n"
    "s_cselect_b32 s70, s57, s70\n"
    "s_cselect_b32 s71, s79, s71\n"
    "s_waitcnt vmcnt(45)\n"
    "s_barrier\n"
    "s_sub_u32 s72, s72, 1\n"
    "s_cbranch_scc0 .Llf_%=\n"
    "s_branch .Lrset_%=\n"
    ".Llidle_%=:\n"
    "s_lshr_b32 s78, s24, 1\n"
    "s_lshl1_add_u32 s78, s27, s78\n"
    "s_mul_i32 s79, s78, 57344\n"
    "s_add_u32 s79, s79, s59\n"
    "s_waitcnt lgkmcnt(0)\n"
    "s_mov_b32 s36, s16\n"
    "s_and_b32 s37, s17, 0xffff\n"
    "s_mov_b32 s38, 1605632\n"
    "s_mov_b32 s39, 0x20000\n"
    "s_add_u32 s78, s79, 0\n"
    "s_add_u32 m0, s59, 81920\n"
    "s_nop 0\n"
    "buffer_load_dwordx4 v16, s[36:39], s78 offen lds\n"
    "buffer_load_dwordx4 v16, s[36:39], s78 offen offset:1024 lds\n"
    "buffer_load_dwordx4 v16, s[36:39], s78 offen offset:2048 lds\n"
    "buffer_load_dwordx4 v16, s[36:39], s78 offen offset:3072 lds\n"
    "s_add_u32 m0, s59, 86016\n"
    "s_add_u32 s78, s78, 4096\n"
    "buffer_load_dwordx4 v16, s[36:39], s78 offen lds\n"
    "buffer_load_dwordx4 v16, s[36:39], s78 offen offset:1024 lds\n"
    "buffer_load_dwordx4 v16, s[36:39], s78 offen offset:2048 lds\n"
    "s_add_u32 s78, s79, 14336\n"
    "s_add_u32 m0, s59, 96256\n"
    "s_nop 0\n"
    "buffer_load_dwordx4 v16, s[36:39], s78 offen lds\n"
    "buffer_load_dwordx4 v16, s[36:39], s78 offen offset:1024 lds\n"
    "buffer_load_dwordx4 v16, s[36:39], s78 offen offset:2048 lds\n"
    "buffer_load_dwordx4 v16, s[36:39], s78 offen offset:3072 lds\n"
    "s_add_u32 m0, s59, 100352\n"
    "s_add_u32 s78, s78, 4096\n"
    "buffer_load_dwordx4 v16, s[36:39], s78 offen lds\n"
    "buffer_load_dwordx4 v16, s[36:39], s78 offen offset:1024 lds\n"
    "buffer_load_dwordx4 v16, s[36:39], s78 offen offset:2048 lds\n"
    "s_add_u32 s78, s79, 28672\n"
    "s_add_u32 m0, s59, 110592\n"
    "s_nop 0\n"
    "buffer_load_dwordx4 v16, s[36:39], s78 offen lds\n"
    "buffer_load_dwordx4 v16, s[36:39], s78 offen offset:1024 lds\n"
    "buffer_load_dwordx4 v16, s[36:39], s78 offen offset:2048 lds\n"
    "buffer_load_dwordx4 v16, s[36:39], s78 offen offset:3072 lds\n"
    "s_add_u32 m0, s59, 114688\n"
    "s_add_u32 s78, s78, 4096\n"
    "buffer_load_dwordx4 v16, s[36:39], s78 offen lds\n"
    "buffer_load_dwordx4 v16, s[36:39], s78 offen offset:1024 lds\n"
    "buffer_load_dwordx4 v16, s[36:39], s78 offen offset:2048 lds\n"
    "s_add_u32 s78, s79, 43008\n"
    "s_add_u32 m0, s59, 124928\n"
    "s_nop 0\n"
    "buffer_load_dwordx4 v16, s[36:39], s78 offen lds\n"
    "buffer_load_dwordx4 v16, s[36:39], s78 offen offset:1024 lds\n"
    "buffer_load_dwordx4 v16, s[36:39], s78 offen offset:2048 lds\n"
    "buffer_load_dwordx4 v16, s[36:39], s78 offen offset:3072 lds\n"
    "s_add_u32 m0, s59, 129024\n"
    "s_add_u32 s78, s78, 4096\n"
    "buffer_load_dwordx4 v16, s[36:39], s78 offen lds\n"
    "buffer_load_dwordx4 v16, s[36:39], s78 offen offset:1024 lds\n"
    "buffer_load_dwordx4 v16, s[36:39], s78 offen offset:2048 lds\n"
    "v_lshrrev_b32 v1, 3, v0\n"
    "v_mul_u32_u24 v3, 4096, v1\n"
    "v_add_u32 v8, v3, v2\n"
    "v_add_u32 v3, 31744, v3\n"
    "v_xor_b32 v9, 32, v2\n"
    "v_add_u32 v9, v3, v9\n"
    "v_add_u32 v3, 31744, v3\n"
    "v_xor_b32 v10, 64, v2\n"
    "v_add_u32 v10, v3, v10\n"
    "v_add_u32 v3, 31744, v3\n"
    "v_xor_b32 v11, 96, v2\n"
    "v_add_u32 v11, v3, v11\n"
    "v_lshrrev_b32 v1, 4, v0\n"
    "v_and_b32 v17, 15, v0\n"
    "v_lshlrev_b32 v18, 1, v1\n"
    "v_xor_b32 v17, v17, v18\n"
    "v_lshlrev_b32 v17, 4, v17\n"
    "v_mul_u32_u24 v3, 3584, v1\n"
    "v_add_u32 v12, v3, v17\n"
    "v_add_u32 v3, 13312, v3\n"
    "v_xor_b32 v13, 0, v17\n"
    "v_add_u32 v13, v3, v13\n"
    "v_add_u32 v3, 13312, v3\n"
    "v_xor_b32 v14, 128, v17\n"
    "v_add_u32 v14, v3, v14\n"
    "v_add_u32 v3, 13312, v3\n"
    "v_xor_b32 v15, 128, v17\n"
    "v_add_u32 v15, v3, v15\n"
    "s_mov_b32 s44, s14\n"
    "s_and_b32 s45, s15, 0xffff\n"
    "s_mov_b32 s46, 1605632\n"
    "s_mov_b32 s47, 0x20000\n"
    "s_mov_b32 s52, s20\n"
    "s_and_b32 s53, s21, 0xffff\n"
    "s_mov_b32 s54, 25165824\n"
    "s_mov_b32 s55, 0x20000\n"
    "s_cmp_ge_u32 s24, 2\n"
    "s_cselect_b32 s48, s8, s6\n"
    "s_cselect_b32 s49, s9, s7\n"
    "s_and_b32 s49, s49, 0xffff\n"
    "s_mov_b32 s50, 8388608\n"
    "s_mov_b32 s51, 0x20000\n"
    "s_mul_i32 s64, s56, 114688\n"
    "s_lshl_b32 s78, s28, 8\n"
    "s_add_u32 s64, s64, s78\n"
    "s_lshl1_add_u32 s65, s27, s56\n"
    "s_lshl_b32 s65, s65, 18\n"
    "s_and_b32 s78, s24, 1\n"
    "s_lshl_b32 s79, s78, 11\n"
    "s_add_u32 s65, s65, s79\n"
    "s_mul_i32 s66, s26, 256\n"
    "s_mul_i32 s68, s25, 0\n"
    "s_add_u32 s66, s66, s68\n"
    "s_lshr_b32 s68, s24, 1\n"
    "s_lshl1_add_u32 s68, s68, s23\n"
    "s_mul_i32 s68, s68, 0\n"
    "s_add_u32 s66, s66, s68\n"
    "s_and_b32 s66, s66, 1023\n"
    "s_mov_b32 s68, s66\n"
    "s_mov_b32 s80, 0\n"
    "s_mov_b32 s81, 0\n"
    "s_lshr_b32 s79, s24, 1\n"
    "s_lshl2_add_u32 s79, s79, s26\n"
    "s_lshl1_add_u32 s79, s79, s78\n"
    "s_mul_i32 s67, s79, 229376\n"
    "s_add_u32 s67, s67, s59\n"
    "s_add_u32 s67, s67, 1048576\n"
    "s_mul_i32 s78, s26, 28\n"
    "s_lshl1_add_u32 s69, s28, s78\n"
    "s_mul_i32 s69, s69, 14336\n"
    "s_add_u32 s69, s69, s59\n"
    "s_add_u32 s69, s69, 5242880\n"
    "s_mul_i32 s79, s24, 7\n"
    "s_add_u32 s74, s78, s79\n"
    "s_mul_i32 s74, s74, 14336\n"
    "s_add_u32 s74, s74, s59\n"
    "s_add_u32 s74, s74, 5242880\n"
    "s_mul_i32 s73, s27, 28\n"
    "s_add_u32 s73, s73, s79\n"
    "s_lshl_b32 s73, s73, 14\n"
    "s_add_u32 s73, s73, s57\n"
    "s_add_u32 s73, s73, 7340032\n"
    "s_lshl1_add_u32 s77, s28, s30\n"
    "s_sub_u32 s77, s77, s79\n"
    "s_sub_u32 s76, 7, s30\n"
    "s_cmp_ge_u32 s25, s76\n"
    "s_cselect_b32 s78, s76, 0\n"
    "s_sub_u32 s75, s25, s78\n"
    "s_add_u32 s70, s57, 0\n"
    "s_add_u32 s71, s59, 81920\n"
    "s_mul_i32 s78, s26, 0x11111111\n"
    "s_mul_i32 s84, s25, 0\n"
    "s_add_u32 s80, s78, 0x32132100\n"
    "s_and_b32 s80, s80, 0x33333333\n"
    "s_or_b32 s80, s80, 0x44400040\n"
    "s_add_u32 s81, s78, 0x32132100\n"
    "s_and_b32 s81, s81, 0x33333333\n"
    "s_or_b32 s81, s81, 0xccc888c8\n"
    "s_mov_b64 s[82:83], s[80:81]\n"
    "s_and_b32 s66, s80, 15\n"
    "s_lshl_b32 s66, s66, 7\n"
    "s_lshr_b64 s[80:81], s[80:81], 4\n"
    "s_and_b32 s68, s82, 15\n"
    "s_lshl_b32 s68, s68, 7\n"
    "s_lshr_b64 s[82:83], s[82:83], 4\n"
    "s_waitcnt vmcnt(0)\n"
    "s_barrier\n"
    "s_barrier\n"
    "s_add_u32 m0, s57, 0\n"
    "s_add_u32 s78, s65, s66\n"
    "buffer_load_dwordx4 v8, s[48:51], s78 offen lds\n"
    "buffer_load_dwordx4 v9, s[48:51], s78 offen offset:1024 lds\n"
    "buffer_load_dwordx4 v10, s[48:51], s78 offen offset:2048 lds\n"
    "buffer_load_dwordx4 v11, s[48:51], s78 offen offset:3072 lds\n"
    "s_add_u32 m0, s57, 4096\n"
    "s_add_u32 s78, s78, 131072\n"
    "buffer_load_dwordx4 v8, s[48:51], s78 offen lds\n"
    "buffer_load_dwordx4 v9, s[48:51], s78 offen offset:1024 lds\n"
    "buffer_load_dwordx4 v10, s[48:51], s78 offen offset:2048 lds\n"
    "buffer_load_dwordx4 v11, s[48:51], s78 offen offset:3072 lds\n"
    "s_and_b32 s66, s80, 15\n"
    "s_lshl_b32 s66, s66, 7\n"
    "s_lshr_b64 s[80:81], s[80:81], 4\n"
    "s_add_u32 m0, s57, 16384\n"
    "s_add_u32 s78, s65, s66\n"
    "buffer_load_dwordx4 v8, s[48:51], s78 offen lds\n"
    "buffer_load_dwordx4 v9, s[48:51], s78 offen offset:1024 lds\n"
    "buffer_load_dwordx4 v10, s[48:51], s78 offen offset:2048 lds\n"
    "buffer_load_dwordx4 v11, s[48:51], s78 offen offset:3072 lds\n"
    "s_add_u32 m0, s57, 20480\n"
    "s_add_u32 s78, s78, 131072\n"
    "buffer_load_dwordx4 v8, s[48:51], s78 offen lds\n"
    "buffer_load_dwordx4 v9, s[48:51], s78 offen offset:1024 lds\n"
    "buffer_load_dwordx4 v10, s[48:51], s78 offen offset:2048 lds\n"
    "buffer_load_dwordx4 v11, s[48:51], s78 offen offset:3072 lds\n"
    "s_and_b32 s66, s80, 15\n"
    "s_lshl_b32 s66, s66, 7\n"
    "s_lshr_b64 s[80:81], s[80:81], 4\n"
    "s_add_u32 m0, s57, 32768\n"
    "s_add_u32 s78, s65, s66\n"
    "buffer_load_dwordx4 v8, s[48:51], s78 offen lds\n"
    "buffer_load_dwordx4 v9, s[48:51], s78 offen offset:1024 lds\n"
    "buffer_load_dwordx4 v10, s[48:51], s78 offen offset:2048 lds\n"
    "buffer_load_dwordx4 v11, s[48:51], s78 offen offset:3072 lds\n"
    "s_add_u32 m0, s57, 36864\n"
    "s_add_u32 s78, s78, 131072\n"
    "buffer_load_dwordx4 v8, s[48:51], s78 offen lds\n"
    "buffer_load_dwordx4 v9, s[48:51], s78 offen offset:1024 lds\n"
    "buffer_load_dwordx4 v10, s[48:51], s78 offen offset:2048 lds\n"
    "buffer_load_dwordx4 v11, s[48:51], s78 offen offset:3072 lds\n"
    "s_and_b32 s66, s80, 15\n"
    "s_lshl_b32 s66, s66, 7\n"
    "s_lshr_b64 s[80:81], s[80:81], 4\n"
    "s_add_u32 m0, s57, 49152\n"
    "s_add_u32 s78, s65, s66\n"
    "buffer_load_dwordx4 v8, s[48:51], s78 offen lds\n"
    "buffer_load_dwordx4 v9, s[48:51], s78 offen offset:1024 lds\n"
    "buffer_load_dwordx4 v10, s[48:51], s78 offen offset:2048 lds\n"
    "buffer_load_dwordx4 v11, s[48:51], s78 offen offset:3072 lds\n"
    "s_add_u32 m0, s57, 53248\n"
    "s_add_u32 s78, s78, 131072\n"
    "buffer_load_dwordx4 v8, s[48:51], s78 offen lds\n"
    "buffer_load_dwordx4 v9, s[48:51], s78 offen offset:1024 lds\n"
    "buffer_load_dwordx4 v10, s[48:51], s78 offen offset:2048 lds\n"
    "buffer_load_dwordx4 v11, s[48:51], s78 offen offset:3072 lds\n"
    "s_and_b32 s66, s80, 15\n"
    "s_lshl_b32 s66, s66, 7\n"
    "s_lshr_b64 s[80:81], s[80:81], 4\n"
    "s_add_u32 m0, s57, 65536\n"
    "s_add_u32 s78, s65, s66\n"
    "buffer_load_dwordx4 v8, s[48:51], s78 offen lds\n"
    "buffer_load_dwordx4 v9, s[48:51], s78 offen offset:1024 lds\n"
    "buffer_load_dwordx4 v10, s[48:51], s78 offen offset:2048 lds\n"
    "buffer_load_dwordx4 v11, s[48:51], s78 offen offset:3072 lds\n"
    "s_add_u32 m0, s57, 69632\n"
    "s_add_u32 s78, s78, 131072\n"
    "buffer_load_dwordx4 v8, s[48:51], s78 offen lds\n"
    "buffer_load_dwordx4 v9, s[48:51], s78 offen offset:1024 lds\n"
    "buffer_load_dwordx4 v10, s[48:51], s78 offen offset:2048 lds\n"
    "buffer_load_dwordx4 v11, s[48:51], s78 offen offset:3072 lds\n"
    "s_and_b32 s66, s80, 15\n"
    "s_lshl_b32 s66, s66, 7\n"
    "s_lshr_b64 s[80:81], s[80:81], 4\n"
    "s_barrier\n"
    "s_branch .Lljoin_%=\n"
    ".Lcmp_%=:\n"
    "s_setprio 0\n"
    "v_and_b32 v1, 15, v0\n"
    "v_lshrrev_b32 v2, 4, v0\n"
    "v_bfe_u32 v3, v0, 2, 2\n"
    "v_and_b32 v4, 3, v0\n"
    "v_bfe_u32 v5, v0, 1, 3\n"
    "v_xor_b32 v5, v5, v2\n"
    "v_lshlrev_b32 v5, 4, v5\n"
    "v_lshl_add_u32 v5, v4, 7, v5\n"
    "v_lshl_add_u32 v5, v3, 10, v5\n"
    "s_lshl_b32 s31, s22, 12\n"
    "v_add_u32 v120, s31, v5\n"
    "v_lshlrev_b32 v122, 4, v0\n"
    "v_lshrrev_b32 v6, 5, v0\n"
    "v_and_b32 v7, 31, v0\n"
    "v_lshlrev_b32 v7, 4, v7\n"
    "v_lshl_add_u32 v7, v6, 10, v7\n"
    "v_add_u32 v123, s31, v7\n"
    "s_lshr_b32 s32, s22, 1\n"
    "s_lshl_b32 s32, s32, 12\n"
    "s_and_b32 s33, s22, 1\n"
    "s_lshl_b32 s33, s33, 9\n"
    "s_add_u32 s32, s32, s33\n"
    "s_add_u32 s32, s32, 81920\n"
    "v_add_u32 v92, s32, v5\n"
    "v_and_b32 v6, 1, v2\n"
    "v_lshl_or_b32 v6, v6, 2, v3\n"
    "s_lshl_b32 s32, s22, 1\n"
    "v_xor_b32 v6, s32, v6\n"
    "v_lshlrev_b32 v6, 5, v6\n"
    "v_lshl_add_u32 v6, v4, 3, v6\n"
    "v_lshl_add_u32 v7, v2, 3, v3\n"
    "v_lshl_add_u32 v6, v7, 8, v6\n"
    "v_add_u32 v121, 32768, v6\n"
    "s_lshl_b32 s32, s22, 6\n"
    "s_add_u32 s32, s32, 153600\n"
    "v_lshl_add_u32 v124, v1, 2, s32\n"
    "s_lshl_b32 s32, s22, 7\n"
    "s_add_u32 s32, s32, 154112\n"
    "v_lshl_add_u32 v125, v2, 4, s32\n"
    "s_mov_b32 s3, s22\n"
    "s_and_b32 s31, s26, 1\n"
    "s_lshl1_add_u32 s32, s24, s31\n"
    "s_cmp_eq_u32 s32, 7\n"
    "s_cselect_b32 s32, 0, s32\n"
    "s_cselect_b32 s33, 8, 0\n"
    "s_mul_i32 s31, s26, 7\n"
    "s_add_u32 s31, s31, s32\n"
    "s_lshr_b32 s31, s31, 1\n"
    "s_lshl_b32 s31, s31, 6\n"
    "s_add_u32 s31, s31, s33\n"
    "s_lshl2_add_u32 s32, s27, s22\n"
    "s_mul_i32 s32, s32, 28672\n"
    "s_add_u32 s31, s31, s32\n"
    "v_lshl_add_u32 v6, v3, 3, v4\n"
    "v_mul_u32_u24 v6, 896, v6\n"
    "v_lshl_add_u32 v6, v2, 4, v6\n"
    "v_add_u32 v126, s31, v6\n"
    "s_mul_i32 s33, s22, 2304\n"
    "s_add_u32 s33, s33, 15728640\n"
    "s_andn2_b32 s34, s2, 6\n"
    "s_add_u32 s31, s24, 1\n"
    "s_and_b32 s31, s31, 3\n"
    "s_lshl2_add_u32 s32, s2, s31\n"
    "s_mul_i32 s32, s32, 9216\n"
    "s_add_u32 s70, s32, s33\n"
    "s_lshl1_add_u32 s31, s31, s34\n"
    "s_lshl2_add_u32 s31, s31, s24\n"
    "s_mul_i32 s31, s31, 9216\n"
    "s_add_u32 s72, s31, s33\n"
    "s_add_u32 s31, s24, 2\n"
    "s_and_b32 s31, s31, 3\n"
    "s_lshl2_add_u32 s32, s2, s31\n"
    "s_mul_i32 s32, s32, 9216\n"
    "s_add_u32 s69, s32, s33\n"
    "s_lshl1_add_u32 s31, s31, s34\n"
    "s_lshl2_add_u32 s31, s31, s24\n"
    "s_mul_i32 s31, s31, 9216\n"
    "s_add_u32 s73, s31, s33\n"
    "s_add_u32 s31, s24, 3\n"
    "s_and_b32 s31, s31, 3\n"
    "s_lshl2_add_u32 s32, s2, s31\n"
    "s_mul_i32 s32, s32, 9216\n"
    "s_add_u32 s68, s32, s33\n"
    "s_lshl1_add_u32 s31, s31, s34\n"
    "s_lshl2_add_u32 s31, s31, s24\n"
    "s_mul_i32 s31, s31, 9216\n"
    "s_add_u32 s74, s31, s33\n"
    "s_sub_u32 s78, 19, s30\n"
    "s_waitcnt lgkmcnt(0)\n"
    "s_mov_b32 s52, s20\n"
    "s_and_b32 s53, s21, 0xffff\n"
    "s_mov_b32 s54, 25165824\n"
    "s_mov_b32 s55, 0x20000\n"
    "s_mov_b32 s36, s4\n"
    "s_and_b32 s37, s5, 0xffff\n"
    "s_mov_b32 s38, 1835008\n"
    "s_mov_b32 s39, 0x20000\n"
    "s_mov_b32 s40, s18\n"
    "s_and_b32 s41, s19, 0xffff\n"
    "s_mov_b32 s42, 1835008\n"
    "s_mov_b32 s43, 0x20000\n"
    "s_cmp_eq_u32 s29, 1\n"
    "s_cbranch_scc1 .Lcidle_%=\n"
    "s_cmp_ge_u32 s28, 7\n"
    "s_cselect_b32 s71, -1.0, 1.0\n"
    "s_cselect_b32 s31, 7, 0\n"
    "s_cselect_b32 s34, 256, 0\n"
    "s_sub_u32 s35, s28, s31\n"
    "s_lshl2_add_u32 s34, s27, s34\n"
    "s_add_u32 s34, s34, s22\n"
    "s_xor_b32 s31, s22, 0\n"
    "s_lshl2_add_u32 s31, s35, s31\n"
    "s_mul_i32 s32, s31, 37\n"
    "s_lshr_b32 s32, s32, 8\n"
    "s_mul_i32 s33, s32, 63\n"
    "s_add_u32 s33, s33, s34\n"
    "s_mul_i32 s33, s33, 7\n"
    "s_add_u32 s33, s33, s31\n"
    "s_mul_i32 s83, s32, 7\n"
    "s_cmp_eq_u32 s83, s31\n"
    "s_cselect_b32 s31, 6, -1\n"
    "s_bitcmp1_b32 s32, 0\n"
    "s_cselect_b32 s31, s31, 0\n"
    "s_add_u32 s33, s33, s31\n"
    "s_lshl_b32 s33, s33, 10\n"
    "s_add_u32 s56, s33, 1048576\n"
    "s_xor_b32 s31, s22, 1\n"
    "s_lshl2_add_u32 s31, s35, s31\n"
    "s_mul_i32 s32, s31, 37\n"
    "s_lshr_b32 s32, s32, 8\n"
    "s_mul_i32 s33, s32, 63\n"
    "s_add_u32 s33, s33, s34\n"
    "s_mul_i32 s33, s33, 7\n"
    "s_add_u32 s33, s33, s31\n"
    "s_mul_i32 s83, s32, 7\n"
    "s_cmp_eq_u32 s83, s31\n"
    "s_cselect_b32 s31, 6, -1\n"
    "s_bitcmp1_b32 s32, 0\n"
    "s_cselect_b32 s31, s31, 0\n"
    "s_add_u32 s33, s33, s31\n"
    "s_lshl_b32 s33, s33, 10\n"
    "s_add_u32 s57, s33, 1048576\n"
    "s_xor_b32 s31, s22, 2\n"
    "s_lshl2_add_u32 s31, s35, s31\n"
    "s_mul_i32 s32, s31, 37\n"
    "s_lshr_b32 s32, s32, 8\n"
    "s_mul_i32 s33, s32, 63\n"
    "s_add_u32 s33, s33, s34\n"
    "s_mul_i32 s33, s33, 7\n"
    "s_add_u32 s33, s33, s31\n"
    "s_mul_i32 s83, s32, 7\n"
    "s_cmp_eq_u32 s83, s31\n"
    "s_cselect_b32 s31, 6, -1\n"
    "s_bitcmp1_b32 s32, 0\n"
    "s_cselect_b32 s31, s31, 0\n"
    "s_add_u32 s33, s33, s31\n"
    "s_lshl_b32 s33, s33, 10\n"
    "s_add_u32 s58, s33, 1048576\n"
    "s_xor_b32 s31, s22, 3\n"
    "s_lshl2_add_u32 s31, s35, s31\n"
    "s_mul_i32 s32, s31, 37\n"
    "s_lshr_b32 s32, s32, 8\n"
    "s_mul_i32 s33, s32, 63\n"
    "s_add_u32 s33, s33, s34\n"
    "s_mul_i32 s33, s33, 7\n"
    "s_add_u32 s33, s33, s31\n"
    "s_mul_i32 s83, s32, 7\n"
    "s_cmp_eq_u32 s83, s31\n"
    "s_cselect_b32 s31, 6, -1\n"
    "s_bitcmp1_b32 s32, 0\n"
    "s_cselect_b32 s31, s31, 0\n"
    "s_add_u32 s33, s33, s31\n"
    "s_lshl_b32 s33, s33, 10\n"
    "s_add_u32 s59, s33, 1048576\n"
    "s_mul_i32 s34, s27, 28\n"
    "s_lshl1_add_u32 s34, s28, s34\n"
    "s_lshl_b32 s34, s34, 14\n"
    "s_add_u32 s34, s34, 7340032\n"
    "s_and_b32 s35, s22, 1\n"
    "s_cmp_eq_u32 s30, 1\n"
    "s_cselect_b32 s35, s35, s22\n"
    "s_xor_b32 s31, s22, 0\n"
    "s_lshr_b32 s32, s31, 1\n"
    "s_lshl_b32 s32, s32, 14\n"
    "s_and_b32 s31, s31, 1\n"
    "s_lshl_b32 s31, s31, 11\n"
    "s_add_u32 s31, s31, s32\n"
    "s_add_u32 s60, s31, s34\n"
    "s_xor_b32 s31, s35, 0\n"
    "s_mul_i32 s31, s31, 7168\n"
    "s_add_u32 s64, s31, 124928\n"
    "s_xor_b32 s31, s22, 1\n"
    "s_lshr_b32 s32, s31, 1\n"
    "s_lshl_b32 s32, s32, 14\n"
    "s_and_b32 s31, s31, 1\n"
    "s_lshl_b32 s31, s31, 11\n"
    "s_add_u32 s31, s31, s32\n"
    "s_add_u32 s61, s31, s34\n"
    "s_xor_b32 s31, s35, 1\n"
    "s_mul_i32 s31, s31, 7168\n"
    "s_add_u32 s65, s31, 124928\n"
    "s_xor_b32 s31, s22, 2\n"
    "s_lshr_b32 s32, s31, 1\n"
    "s_lshl_b32 s32, s32, 14\n"
    "s_and_b32 s31, s31, 1\n"
    "s_lshl_b32 s31, s31, 11\n"
    "s_add_u32 s31, s31, s32\n"
    "s_add_u32 s62, s31, s34\n"
    "s_xor_b32 s31, s35, 2\n"
    "s_mul_i32 s31, s31, 7168\n"
    "s_add_u32 s66, s31, 124928\n"
    "s_xor_b32 s31, s22, 3\n"
    "s_lshr_b32 s32, s31, 1\n"
    "s_lshl_b32 s32, s32, 14\n"
    "s_and_b32 s31, s31, 1\n"
    "s_lshl_b32 s31, s31, 11\n"
    "s_add_u32 s31, s31, s32\n"
    "s_add_u32 s63, s31, s34\n"
    "s_xor_b32 s31, s35, 3\n"
    "s_mul_i32 s31, s31, 7168\n"
    "s_add_u32 s67, s31, 124928\n"
    "s_mov_b32 s84, 0xaaaaaaaa\n"
    "s_mov_b32 s85, 0xaaaaaaaa\n"
    "s_mov_b32 s86, 0xcccccccc\n"
    "s_mov_b32 s87, 0xcccccccc\n"
    "s_mov_b32 s88, 0xf0f0f0f0\n"
    "s_mov_b32 s89, 0xf0f0f0f0\n"
    "s_mov_b32 s90, 0xff00ff00\n"
    "s_mov_b32 s91, 0xff00ff00\n"
    "v_mov_b32 v84, 0x3f803f80\n"
    "v_mov_b32 v85, 0x3f803f80\n"
    "v_mov_b32 v86, 0x3f803f80\n"
    "v_mov_b32 v87, 0x3f803f80\n"
    "v_xor_b32 v89, 64, v120\n"
    "v_add_u32 v90, 0x10000, v120\n"
    "v_add_u32 v91, 0x10000, v89\n"
    "v_mov_b32 v88, 0x3b124925\n"
    "v_xor_b32 v93, 512, v92\n"
    "v_xor_b32 v94, 4096, v92\n"
    "v_xor_b32 v95, 4608, v92\n"
    "v_xor_b32 v96, 64, v92\n"
    "v_xor_b32 v97, 576, v92\n"
    "v_xor_b32 v98, 4160, v92\n"
    "v_xor_b32 v99, 4672, v92\n"
    "v_xor_b32 v108, 0, v121\n"
    "v_xor_b32 v109, 32, v121\n"
    "v_xor_b32 v110, 64, v121\n"
    "v_xor_b32 v111, 96, v121\n"
    "v_xor_b32 v112, 128, v121\n"
    "v_xor_b32 v113, 160, v121\n"
    "v_xor_b32 v114, 192, v121\n"
    "v_xor_b32 v115, 224, v121\n"
    "v_xor_b32 v101, 64, v124\n"
    "v_xor_b32 v102, 128, v124\n"
    "v_xor_b32 v103, 192, v124\n"
    "v_mov_b32 v104, 0x3f803f80\n"
    "v_mov_b32 v105, 0x3f803f80\n"
    "v_mov_b32 v106, 0x3f803f80\n"
    "v_mov_b32 v107, 0x3f803f80\n"
    "s_barrier\n"
    "ds_read_b128 v[144:147], v120 offset:0\n"
    "ds_read_b128 v[52:55], v92 offset:49152\n"
    "ds_read_b128 v[148:151], v120 offset:512\n"
    "ds_read_b128 v[56:59], v93 offset:49152\n"
    "ds_read_b128 v[60:63], v94 offset:49152\n"
    "ds_read_b128 v[64:67], v95 offset:49152\n"
    "s_waitcnt lgkmcnt(3)\n"
    "v_mfma_f32_16x16x32_bf16 v[0:3], v[144:147], v[52:55], 0\n"
    "ds_read_b128 v[152:155], v89 offset:0\n"
    "v_mfma_f32_16x16x32_bf16 v[4:7], v[148:151], v[52:55], 0\n"
    "ds_read_b128 v[68:71], v96 offset:49152\n"
    "v_mfma_f32_16x16x32_bf16 v[48:51], v[84:87], v[52:55], 0\n"
    "ds_read_b128 v[156:159], v89 offset:512\n"
    "v_mfma_f32_16x16x32_bf16 v[32:35], v[144:147], v[144:147], 0\n"
    "ds_read_b128 v[72:75], v97 offset:49152\n"
    "v_mfma_f32_16x16x32_bf16 v[40:43], v[144:147], v[84:87], 0\n"
    "ds_read_b128 v[76:79], v98 offset:49152\n"
    "s_waitcnt lgkmcnt(5)\n"
    "v_mfma_f32_16x16x32_bf16 v[8:11], v[144:147], v[56:59], 0\n"
    "ds_read_b128 v[80:83], v99 offset:49152\n"
    "v_mfma_f32_16x16x32_bf16 v[12:15], v[148:151], v[56:59], 0\n"
    "v_mfma_f32_16x16x32_bf16 v[36:39], v[148:151], v[148:151], 0\n"
    "v_mfma_f32_16x16x32_bf16 v[44:47], v[148:151], v[84:87], 0\n"
    "v_mfma_f32_16x16x32_bf16 v[16:19], v[144:147], v[60:63], 0\n"
    "v_mfma_f32_16x16x32_bf16 v[20:23], v[148:151], v[60:63], 0\n"
    "v_mfma_f32_16x16x32_bf16 v[24:27], v[144:147], v[64:67], 0\n"
    "v_mfma_f32_16x16x32_bf16 v[28:31], v[148:151], v[64:67], 0\n"
    "s_waitcnt lgkmcnt(0)\n"
    "s_barrier\n"
    "v_mfma_f32_16x16x32_bf16 v[0:3], v[152:155], v[68:71], v[0:3]\n"
    "ds_read_b128 v[160:163], v120 offset:16384\n"
    "v_mfma_f32_16x16x32_bf16 v[4:7], v[156:159], v[68:71], v[4:7]\n"
    "ds_read_b128 v[52:55], v92 offset:57344\n"
    "v_mfma_f32_16x16x32_bf16 v[48:51], v[84:87], v[68:71], v[48:51]\n"
    "ds_read_b128 v[164:167], v120 offset:16896\n"
    "v_mfma_f32_16x16x32_bf16 v[32:35], v[152:155], v[152:155], v[32:35]\n"
    "ds_read_b128 v[56:59], v93 offset:57344\n"
    "v_mfma_f32_16x16x32_bf16 v[40:43], v[152:155], v[84:87], v[40:43]\n"
    "ds_read_b128 v[60:63], v94 offset:57344\n"
    "v_mfma_f32_16x16x32_bf16 v[8:11], v[152:155], v[72:75], v[8:11]\n"
    "ds_read_b128 v[64:67], v95 offset:57344\n"
    "v_mfma_f32_16x16x32_bf16 v[12:15], v[156:159], v[72:75], v[12:15]\n"
    "v_mfma_f32_16x16x32_bf16 v[36:39], v[156:159], v[156:159], v[36:39]\n"
    "v_mfma_f32_16x16x32_bf16 v[44:47], v[156:159], v[84:87], v[44:47]\n"
    "v_mfma_f32_16x16x32_bf16 v[16:19], v[152:155], v[76:79], v[16:19]\n"
    "v_mfma_f32_16x16x32_bf16 v[20:23], v[156:159], v[76:79], v[20:23]\n"
    "v_mfma_f32_16x16x32_bf16 v[24:27], v[152:155], v[80:83], v[24:27]\n"
    "v_mfma_f32_16x16x32_bf16 v[28:31], v[156:159], v[80:83], v[28:31]\n"
    "s_waitcnt lgkmcnt(3)\n"
    "v_mfma_f32_16x16x32_bf16 v[0:3], v[160:163], v[52:55], v[0:3]\n"
    "ds_read_b128 v[168:171], v89 offset:16384\n"
    "v_mfma_f32_16x16x32_bf16 v[4:7], v[164:167], v[52:55], v[4:7]\n"
    "ds_read_b128 v[68:71], v96 offset:57344\n"
    "v_mfma_f32_16x16x32_bf16 v[48:51], v[84:87], v[52:55], v[48:51]\n"
    "ds_read_b128 v[172:175], v89 offset:16896\n"
    "v_mfma_f32_16x16x32_bf16 v[32:35], v[160:163], v[160:163], v[32:35]\n"
    "ds_read_b128 v[72:75], v97 offset:57344\n"
    "v_mfma_f32_16x16x32_bf16 v[40:43], v[160:163], v[84:87], v[40:43]\n"
    "ds_read_b128 v[76:79], v98 offset:57344\n"
    "s_waitcnt lgkmcnt(5)\n"
    "v_mfma_f32_16x16x32_bf16 v[8:11], v[160:163], v[56:59], v[8:11]\n"
    "ds_read_b128 v[80:83], v99 offset:57344\n"
    "v_mfma_f32_16x16x32_bf16 v[12:15], v[164:167], v[56:59], v[12:15]\n"
    "v_mfma_f32_16x16x32_bf16 v[36:39], v[164:167], v[164:167], v[36:39]\n"
    "v_mfma_f32_16x16x32_bf16 v[44:47], v[164:167], v[84:87], v[44:47]\n"
    "v_mfma_f32_16x16x32_bf16 v[16:19], v[160:163], v[60:63], v[16:19]\n"
    "v_mfma_f32_16x16x32_bf16 v[20:23], v[164:167], v[60:63], v[20:23]\n"
    "v_mfma_f32_16x16x32_bf16 v[24:27], v[160:163], v[64:67], v[24:27]\n"
    "v_mfma_f32_16x16x32_bf16 v[28:31], v[164:167], v[64:67], v[28:31]\n"
    "s_waitcnt lgkmcnt(0)\n"
    "s_barrier\n"
    "v_mfma_f32_16x16x32_bf16 v[0:3], v[168:171], v[68:71], v[0:3]\n"
    "ds_read_b128 v[176:179], v120 offset:32768\n"
    "v_mfma_f32_16x16x32_bf16 v[4:7], v[172:175], v[68:71], v[4:7]\n"
    "ds_read_b128 v[52:55], v92 offset:32768\n"
    "v_mfma_f32_16x16x32_bf16 v[48:51], v[84:87], v[68:71], v[48:51]\n"
    "ds_read_b128 v[180:183], v120 offset:33280\n"
    "v_mfma_f32_16x16x32_bf16 v[32:35], v[168:171], v[168:171], v[32:35]\n"
    "ds_read_b128 v[56:59], v93 offset:32768\n"
    "v_mfma_f32_16x16x32_bf16 v[40:43], v[168:171], v[84:87], v[40:43]\n"
    "ds_read_b128 v[60:63], v94 offset:32768\n"
    "v_mfma_f32_16x16x32_bf16 v[8:11], v[168:171], v[72:75], v[8:11]\n"
    "ds_read_b128 v[64:67], v95 offset:32768\n"
    "v_mfma_f32_16x16x32_bf16 v[12:15], v[172:175], v[72:75], v[12:15]\n"
    "v_mfma_f32_16x16x32_bf16 v[36:39], v[172:175], v[172:175], v[36:39]\n"
    "v_mfma_f32_16x16x32_bf16 v[44:47], v[172:175], v[84:87], v[44:47]\n"
    "v_mfma_f32_16x16x32_bf16 v[16:19], v[168:171], v[76:79], v[16:19]\n"
    "v_mfma_f32_16x16x32_bf16 v[20:23], v[172:175], v[76:79], v[20:23]\n"
    "v_mfma_f32_16x16x32_bf16 v[24:27], v[168:171], v[80:83], v[24:27]\n"
    "v_mfma_f32_16x16x32_bf16 v[28:31], v[172:175], v[80:83], v[28:31]\n"
    "s_waitcnt lgkmcnt(3)\n"
    "v_mfma_f32_16x16x32_bf16 v[0:3], v[176:179], v[52:55], v[0:3]\n"
    "ds_read_b128 v[184:187], v89 offset:32768\n"
    "v_mfma_f32_16x16x32_bf16 v[4:7], v[180:183], v[52:55], v[4:7]\n"
    "ds_read_b128 v[68:71], v96 offset:32768\n"
    "v_mfma_f32_16x16x32_bf16 v[48:51], v[84:87], v[52:55], v[48:51]\n"
    "ds_read_b128 v[188:191], v89 offset:33280\n"
    "v_mfma_f32_16x16x32_bf16 v[32:35], v[176:179], v[176:179], v[32:35]\n"
    "ds_read_b128 v[72:75], v97 offset:32768\n"
    "v_mfma_f32_16x16x32_bf16 v[40:43], v[176:179], v[84:87], v[40:43]\n"
    "ds_read_b128 v[76:79], v98 offset:32768\n"
    "s_waitcnt lgkmcnt(5)\n"
    "v_mfma_f32_16x16x32_bf16 v[8:11], v[176:179], v[56:59], v[8:11]\n"
    "ds_read_b128 v[80:83], v99 offset:32768\n"
    "v_mfma_f32_16x16x32_bf16 v[12:15], v[180:183], v[56:59], v[12:15]\n"
    "v_mfma_f32_16x16x32_bf16 v[36:39], v[180:183], v[180:183], v[36:39]\n"
    "v_mfma_f32_16x16x32_bf16 v[44:47], v[180:183], v[84:87], v[44:47]\n"
    "v_mfma_f32_16x16x32_bf16 v[16:19], v[176:179], v[60:63], v[16:19]\n"
    "v_mfma_f32_16x16x32_bf16 v[20:23], v[180:183], v[60:63], v[20:23]\n"
    "v_mfma_f32_16x16x32_bf16 v[24:27], v[176:179], v[64:67], v[24:27]\n"
    "v_mfma_f32_16x16x32_bf16 v[28:31], v[180:183], v[64:67], v[28:31]\n"
    "s_waitcnt lgkmcnt(0)\n"
    "s_barrier\n"
    "v_mfma_f32_16x16x32_bf16 v[0:3], v[184:187], v[68:71], v[0:3]\n"
    "ds_read_b128 v[192:195], v120 offset:49152\n"
    "v_mfma_f32_16x16x32_bf16 v[4:7], v[188:191], v[68:71], v[4:7]\n"
    "ds_read_b128 v[52:55], v92 offset:40960\n"
    "v_mfma_f32_16x16x32_bf16 v[48:51], v[84:87], v[68:71], v[48:51]\n"
    "ds_read_b128 v[196:199], v120 offset:49664\n"
    "v_mfma_f32_16x16x32_bf16 v[32:35], v[184:187], v[184:187], v[32:35]\n"
    "ds_read_b128 v[56:59], v93 offset:40960\n"
    "v_mfma_f32_16x16x32_bf16 v[40:43], v[184:187], v[84:87], v[40:43]\n"
    "ds_read_b128 v[60:63], v94 offset:40960\n"
    "v_mfma_f32_16x16x32_bf16 v[8:11], v[184:187], v[72:75], v[8:11]\n"
    "ds_read_b128 v[64:67], v95 offset:40960\n"
    "v_mfma_f32_16x16x32_bf16 v[12:15], v[188:191], v[72:75], v[12:15]\n"
    "v_mfma_f32_16x16x32_bf16 v[36:39], v[188:191], v[188:191], v[36:39]\n"
    "v_mfma_f32_16x16x32_bf16 v[44:47], v[188:191], v[84:87], v[44:47]\n"
    "v_mfma_f32_16x16x32_bf16 v[16:19], v[184:187], v[76:79], v[16:19]\n"
    "v_mfma_f32_16x16x32_bf16 v[20:23], v[188:191], v[76:79], v[20:23]\n"
    "v_mfma_f32_16x16x32_bf16 v[24:27], v[184:187], v[80:83], v[24:27]\n"
    "v_mfma_f32_16x16x32_bf16 v[28:31], v[188:191], v[80:83], v[28:31]\n"
    "s_waitcnt lgkmcnt(3)\n"
    "v_mfma_f32_16x16x32_bf16 v[0:3], v[192:195], v[52:55], v[0:3]\n"
    "ds_read_b128 v[200:203], v89 offset:49152\n"
    "v_mfma_f32_16x16x32_bf16 v[4:7], v[196:199], v[52:55], v[4:7]\n"
    "ds_read_b128 v[68:71], v96 offset:40960\n"
    "v_mfma_f32_16x16x32_bf16 v[48:51], v[84:87], v[52:55], v[48:51]\n"
    "ds_read_b128 v[204:207], v89 offset:49664\n"
    "v_mfma_f32_16x16x32_bf16 v[32:35], v[192:195], v[192:195], v[32:35]\n"
    "ds_read_b128 v[72:75], v97 offset:40960\n"
    "v_mfma_f32_16x16x32_bf16 v[40:43], v[192:195], v[84:87], v[40:43]\n"
    "ds_read_b128 v[76:79], v98 offset:40960\n"
    "s_waitcnt lgkmcnt(5)\n"
    "v_mfma_f32_16x16x32_bf16 v[8:11], v[192:195], v[56:59], v[8:11]\n"
    "ds_read_b128 v[80:83], v99 offset:40960\n"
    "v_mfma_f32_16x16x32_bf16 v[12:15], v[196:199], v[56:59], v[12:15]\n"
    "v_mfma_f32_16x16x32_bf16 v[36:39], v[196:199], v[196:199], v[36:39]\n"
    "v_mfma_f32_16x16x32_bf16 v[44:47], v[196:199], v[84:87], v[44:47]\n"
    "v_mfma_f32_16x16x32_bf16 v[16:19], v[192:195], v[60:63], v[16:19]\n"
    "v_mfma_f32_16x16x32_bf16 v[20:23], v[196:199], v[60:63], v[20:23]\n"
    "v_mfma_f32_16x16x32_bf16 v[24:27], v[192:195], v[64:67], v[24:27]\n"
    "v_mfma_f32_16x16x32_bf16 v[28:31], v[196:199], v[64:67], v[28:31]\n"
    "s_waitcnt lgkmcnt(0)\n"
    "s_barrier\n"
    "v_mfma_f32_16x16x32_bf16 v[0:3], v[200:203], v[68:71], v[0:3]\n"
    "ds_read_b128 v[208:211], v90 offset:0\n"
    "v_mfma_f32_16x16x32_bf16 v[4:7], v[204:207], v[68:71], v[4:7]\n"
    "ds_read_b128 v[52:55], v92 offset:16384\n"
    "v_mfma_f32_16x16x32_bf16 v[48:51], v[84:87], v[68:71], v[48:51]\n"
    "ds_read_b128 v[212:215], v90 offset:512\n"
    "v_mfma_f32_16x16x32_bf16 v[32:35], v[200:203], v[200:203], v[32:35]\n"
    "ds_read_b128 v[56:59], v93 offset:16384\n"
    "v_mfma_f32_16x16x32_bf16 v[40:43], v[200:203], v[84:87], v[40:43]\n"
    "ds_read_b128 v[60:63], v94 offset:16384\n"
    "v_mfma_f32_16x16x32_bf16 v[8:11], v[200:203], v[72:75], v[8:11]\n"
    "ds_read_b128 v[64:67], v95 offset:16384\n"
    "v_mfma_f32_16x16x32_bf16 v[12:15], v[204:207], v[72:75], v[12:15]\n"
    "v_mfma_f32_16x16x32_bf16 v[36:39], v[204:207], v[204:207], v[36:39]\n"
    "v_mfma_f32_16x16x32_bf16 v[44:47], v[204:207], v[84:87], v[44:47]\n"
    "v_mfma_f32_16x16x32_bf16 v[16:19], v[200:203], v[76:79], v[16:19]\n"
    "v_mfma_f32_16x16x32_bf16 v[20:23], v[204:207], v[76:79], v[20:23]\n"
    "v_mfma_f32_16x16x32_bf16 v[24:27], v[200:203], v[80:83], v[24:27]\n"
    "v_mfma_f32_16x16x32_bf16 v[28:31], v[204:207], v[80:83], v[28:31]\n"
    "s_waitcnt lgkmcnt(3)\n"
    "v_mfma_f32_16x16x32_bf16 v[0:3], v[208:211], v[52:55], v[0:3]\n"
    "ds_read_b128 v[216:219], v91 offset:0\n"
    "v_mfma_f32_16x16x32_bf16 v[4:7], v[212:215], v[52:55], v[4:7]\n"
    "ds_read_b128 v[68:71], v96 offset:16384\n"
    "v_mfma_f32_16x16x32_bf16 v[48:51], v[84:87], v[52:55], v[48:51]\n"
    "ds_read_b128 v[220:223], v91 offset:512\n"
    "v_mfma_f32_16x16x32_bf16 v[32:35], v[208:211], v[208:211], v[32:35]\n"
    "ds_read_b128 v[72:75], v97 offset:16384\n"
    "v_mfma_f32_16x16x32_bf16 v[40:43], v[208:211], v[84:87], v[40:43]\n"
    "ds_read_b128 v[76:79], v98 offset:16384\n"
    "s_waitcnt lgkmcnt(5)\n"
    "v_mfma_f32_16x16x32_bf16 v[8:11], v[208:211], v[56:59], v[8:11]\n"
    "ds_read_b128 v[80:83], v99 offset:16384\n"
    "v_mfma_f32_16x16x32_bf16 v[12:15], v[212:215], v[56:59], v[12:15]\n"
    "v_mfma_f32_16x16x32_bf16 v[36:39], v[212:215], v[212:215], v[36:39]\n"
    "v_mfma_f32_16x16x32_bf16 v[44:47], v[212:215], v[84:87], v[44:47]\n"
    "v_mfma_f32_16x16x32_bf16 v[16:19], v[208:211], v[60:63], v[16:19]\n"
    "v_mfma_f32_16x16x32_bf16 v[20:23], v[212:215], v[60:63], v[20:23]\n"
    "v_mfma_f32_16x16x32_bf16 v[24:27], v[208:211], v[64:67], v[24:27]\n"
    "v_mfma_f32_16x16x32_bf16 v[28:31], v[212:215], v[64:67], v[28:31]\n"
    "s_waitcnt lgkmcnt(0)\n"
    "s_barrier\n"
    "v_mfma_f32_16x16x32_bf16 v[0:3], v[216:219], v[68:71], v[0:3]\n"
    "ds_read_b128 v[224:227], v120 offset:0\n"
    "v_mfma_f32_16x16x32_bf16 v[4:7], v[220:223], v[68:71], v[4:7]\n"
    "ds_read_b128 v[52:55], v92 offset:24576\n"
    "v_mfma_f32_16x16x32_bf16 v[48:51], v[84:87], v[68:71], v[48:51]\n"
    "ds_read_b128 v[228:231], v120 offset:512\n"
    "v_mfma_f32_16x16x32_bf16 v[32:35], v[216:219], v[216:219], v[32:35]\n"
    "ds_read_b128 v[56:59], v93 offset:24576\n"
    "v_mfma_f32_16x16x32_bf16 v[40:43], v[216:219], v[84:87], v[40:43]\n"
    "ds_read_b128 v[60:63], v94 offset:24576\n"
    "v_mfma_f32_16x16x32_bf16 v[8:11], v[216:219], v[72:75], v[8:11]\n"
    "ds_read_b128 v[64:67], v95 offset:24576\n"
    "v_mfma_f32_16x16x32_bf16 v[12:15], v[220:223], v[72:75], v[12:15]\n"
    "v_mfma_f32_16x16x32_bf16 v[36:39], v[220:223], v[220:223], v[36:39]\n"
    "v_mfma_f32_16x16x32_bf16 v[44:47], v[220:223], v[84:87], v[44:47]\n"
    "v_mfma_f32_16x16x32_bf16 v[16:19], v[216:219], v[76:79], v[16:19]\n"
    "v_mfma_f32_16x16x32_bf16 v[20:23], v[220:223], v[76:79], v[20:23]\n"
    "v_mfma_f32_16x16x32_bf16 v[24:27], v[216:219], v[80:83], v[24:27]\n"
    "v_mfma_f32_16x16x32_bf16 v[28:31], v[220:223], v[80:83], v[28:31]\n"
    "s_waitcnt lgkmcnt(3)\n"
    "v_mfma_f32_16x16x32_bf16 v[0:3], v[224:227], v[52:55], v[0:3]\n"
    "ds_read_b128 v[232:235], v89 offset:0\n"
    "v_mfma_f32_16x16x32_bf16 v[4:7], v[228:231], v[52:55], v[4:7]\n"
    "ds_read_b128 v[68:71], v96 offset:24576\n"
    "v_mfma_f32_16x16x32_bf16 v[48:51], v[84:87], v[52:55], v[48:51]\n"
    "ds_read_b128 v[236:239], v89 offset:512\n"
    "v_mfma_f32_16x16x32_bf16 v[32:35], v[224:227], v[224:227], v[32:35]\n"
    "ds_read_b128 v[72:75], v97 offset:24576\n"
    "v_mfma_f32_16x16x32_bf16 v[40:43], v[224:227], v[84:87], v[40:43]\n"
    "ds_read_b128 v[76:79], v98 offset:24576\n"
    "s_waitcnt lgkmcnt(5)\n"
    "v_mfma_f32_16x16x32_bf16 v[8:11], v[224:227], v[56:59], v[8:11]\n"
    "ds_read_b128 v[80:83], v99 offset:24576\n"
    "v_mfma_f32_16x16x32_bf16 v[12:15], v[228:231], v[56:59], v[12:15]\n"
    "v_mfma_f32_16x16x32_bf16 v[36:39], v[228:231], v[228:231], v[36:39]\n"
    "v_mfma_f32_16x16x32_bf16 v[44:47], v[228:231], v[84:87], v[44:47]\n"
    "v_mfma_f32_16x16x32_bf16 v[16:19], v[224:227], v[60:63], v[16:19]\n"
    "v_mfma_f32_16x16x32_bf16 v[20:23], v[228:231], v[60:63], v[20:23]\n"
    "v_mfma_f32_16x16x32_bf16 v[24:27], v[224:227], v[64:67], v[24:27]\n"
    "v_mfma_f32_16x16x32_bf16 v[28:31], v[228:231], v[64:67], v[28:31]\n"
    "s_waitcnt lgkmcnt(0)\n"
    "s_barrier\n"
    "v_mfma_f32_16x16x32_bf16 v[0:3], v[232:235], v[68:71], v[0:3]\n"
    "ds_read_b128 v[240:243], v120 offset:16384\n"
    "v_mfma_f32_16x16x32_bf16 v[4:7], v[236:239], v[68:71], v[4:7]\n"
    "ds_read_b128 v[52:55], v92 offset:0\n"
    "v_mfma_f32_16x16x32_bf16 v[48:51], v[84:87], v[68:71], v[48:51]\n"
    "ds_read_b128 v[244:247], v120 offset:16896\n"
    "v_mfma_f32_16x16x32_bf16 v[32:35], v[232:235], v[232:235], v[32:35]\n"
    "ds_read_b128 v[56:59], v93 offset:0\n"
    "v_mfma_f32_16x16x32_bf16 v[40:43], v[232:235], v[84:87], v[40:43]\n"
    "ds_read_b128 v[60:63], v94 offset:0\n"
    "v_mfma_f32_16x16x32_bf16 v[8:11], v[232:235], v[72:75], v[8:11]\n"
    "ds_read_b128 v[64:67], v95 offset:0\n"
    "v_mfma_f32_16x16x32_bf16 v[12:15], v[236:239], v[72:75], v[12:15]\n"
    "v_mfma_f32_16x16x32_bf16 v[36:39], v[236:239], v[236:239], v[36:39]\n"
    "v_mfma_f32_16x16x32_bf16 v[44:47], v[236:239], v[84:87], v[44:47]\n"
    "v_mfma_f32_16x16x32_bf16 v[16:19], v[232:235], v[76:79], v[16:19]\n"
    "v_mfma_f32_16x16x32_bf16 v[20:23], v[236:239], v[76:79], v[20:23]\n"
    "v_mfma_f32_16x16x32_bf16 v[24:27], v[232:235], v[80:83], v[24:27]\n"
    "v_mfma_f32_16x16x32_bf16 v[28:31], v[236:239], v[80:83], v[28:31]\n"
    "s_waitcnt lgkmcnt(3)\n"
    "v_mfma_f32_16x16x32_bf16 v[0:3], v[240:243], v[52:55], v[0:3]\n"
    "ds_read_b128 v[248:251], v89 offset:16384\n"
    "v_mfma_f32_16x16x32_bf16 v[4:7], v[244:247], v[52:55], v[4:7]\n"
    "ds_read_b128 v[68:71], v96 offset:0\n"
    "v_mfma_f32_16x16x32_bf16 v[48:51], v[84:87], v[52:55], v[48:51]\n"
    "ds_read_b128 v[252:255], v89 offset:16896\n"
    "v_mfma_f32_16x16x32_bf16 v[32:35], v[240:243], v[240:243], v[32:35]\n"
    "ds_read_b128 v[72:75], v97 offset:0\n"
    "v_mfma_f32_16x16x32_bf16 v[40:43], v[240:243], v[84:87], v[40:43]\n"
    "ds_read_b128 v[76:79], v98 offset:0\n"
    "s_waitcnt lgkmcnt(5)\n"
    "v_mfma_f32_16x16x32_bf16 v[8:11], v[240:243], v[56:59], v[8:11]\n"
    "ds_read_b128 v[80:83], v99 offset:0\n"
    "v_mfma_f32_16x16x32_bf16 v[12:15], v[244:247], v[56:59], v[12:15]\n"
    "v_mfma_f32_16x16x32_bf16 v[36:39], v[244:247], v[244:247], v[36:39]\n"
    "v_mfma_f32_16x16x32_bf16 v[44:47], v[244:247], v[84:87], v[44:47]\n"
    "v_mfma_f32_16x16x32_bf16 v[16:19], v[240:243], v[60:63], v[16:19]\n"
    "v_mfma_f32_16x16x32_bf16 v[20:23], v[244:247], v[60:63], v[20:23]\n"
    "v_mfma_f32_16x16x32_bf16 v[24:27], v[240:243], v[64:67], v[24:27]\n"
    "v_mfma_f32_16x16x32_bf16 v[28:31], v[244:247], v[64:67], v[28:31]\n"
    "s_waitcnt lgkmcnt(0)\n"
    "v_mfma_f32_16x16x32_bf16 v[48:51], v[84:87], v[68:71], v[48:51]\n"
    "v_mfma_f32_16x16x32_bf16 v[32:35], v[248:251], v[248:251], v[32:35]\n"
    "v_mfma_f32_16x16x32_bf16 v[40:43], v[248:251], v[84:87], v[40:43]\n"
    "v_mfma_f32_16x16x32_bf16 v[36:39], v[252:255], v[252:255], v[36:39]\n"
    "v_mul_f32 v48, 0xbb124925, v48\n"
    "ds_write_b32 v124, v48\n"
    "v_mfma_f32_16x16x32_bf16 v[44:47], v[252:255], v[84:87], v[44:47]\n"
    "v_mfma_f32_16x16x32_bf16 v[0:3], v[248:251], v[68:71], v[0:3]\n"
    "v_cndmask_b32 v32, v32, v33, s[84:85]\n"
    "v_cndmask_b32 v34, v34, v35, s[84:85]\n"
    "v_mfma_f32_16x16x32_bf16 v[4:7], v[252:255], v[68:71], v[4:7]\n"
    "v_cndmask_b32 v52, v40, v41, s[84:85]\n"
    "v_cndmask_b32 v53, v42, v43, s[84:85]\n"
    "v_mfma_f32_16x16x32_bf16 v[8:11], v[248:251], v[72:75], v[8:11]\n"
    "v_cndmask_b32 v32, v32, v34, s[86:87]\n"
    "v_cndmask_b32 v36, v36, v37, s[84:85]\n"
    "v_mfma_f32_16x16x32_bf16 v[12:15], v[252:255], v[72:75], v[12:15]\n"
    "v_cndmask_b32 v52, v52, v53, s[86:87]\n"
    "v_cndmask_b32 v38, v38, v39, s[84:85]\n"
    "v_mfma_f32_16x16x32_bf16 v[16:19], v[248:251], v[76:79], v[16:19]\n"
    "v_mul_f32 v32, v32, v88\n"
    "v_cndmask_b32 v54, v44, v45, s[84:85]\n"
    "v_mfma_f32_16x16x32_bf16 v[20:23], v[252:255], v[76:79], v[20:23]\n"
    "v_mul_f32 v52, v52, v88\n"
    "v_cndmask_b32 v55, v46, v47, s[84:85]\n"
    "v_mfma_f32_16x16x32_bf16 v[24:27], v[248:251], v[80:83], v[24:27]\n"
    "v_fma_f32 v32, -v52, v52, v32\n"
    "v_cndmask_b32 v36, v36, v38, s[86:87]\n"
    "v_mfma_f32_16x16x32_bf16 v[28:31], v[252:255], v[80:83], v[28:31]\n"
    "v_max_f32 v32, 0, v32\n"
    "v_cndmask_b32 v54, v54, v55, s[86:87]\n"
    "v_add_f32 v32, 0x3727c5ac, v32\n"
    "v_mul_f32 v36, v36, v88\n"
    "v_rsq_f32 v116, v32\n"
    "v_mul_f32 v54, v54, v88\n"
    "v_fma_f32 v36, -v54, v54, v36\n"
    "v_max_f32 v36, 0, v36\n"
    "v_add_f32 v36, 0x3727c5ac, v36\n"
    "v_rsq_f32 v118, v36\n"
    "v_mul_f32 v32, s71, v116\n"
    "v_mul_f32 v33, s71, v116\n"
    "v_mul_f32_e64 v117, -v52, v116\n"
    "v_mul_f32 v36, s71, v118\n"
    "v_mul_f32 v37, s71, v118\n"
    "v_mul_f32_e64 v119, -v54, v118\n"
    "v_mov_b32_dpp v33, v32 row_ror:12 row_mask:0x2 bank_mask:0xf\n"
    "v_mov_b32_dpp v37, v36 row_ror:12 row_mask:0x2 bank_mask:0xf\n"
    "s_nop 0\n"
    "v_mov_b32_dpp v33, v32 row_ror:8 row_mask:0x4 bank_mask:0xf\n"
    "v_mov_b32_dpp v37, v36 row_ror:8 row_mask:0x4 bank_mask:0xf\n"
    "s_nop 0\n"
    "v_mov_b32_dpp v33, v32 row_ror:4 row_mask:0x8 bank_mask:0xf\n"
    "v_mov_b32_dpp v37, v36 row_ror:4 row_mask:0x8 bank_mask:0xf\n"
    "s_nop 0\n"
    "v_mov_b32_dpp v128, v33 row_newbcast:0 row_mask:0xf bank_mask:0xf\n"
    "v_mov_b32_dpp v132, v37 row_newbcast:0 row_mask:0xf bank_mask:0xf\n"
    "v_mov_b32_dpp v129, v33 row_newbcast:1 row_mask:0xf bank_mask:0xf\n"
    "v_mov_b32_dpp v133, v37 row_newbcast:1 row_mask:0xf bank_mask:0xf\n"
    "v_mov_b32_dpp v130, v33 row_newbcast:2 row_mask:0xf bank_mask:0xf\n"
    "v_mov_b32_dpp v134, v37 row_newbcast:2 row_mask:0xf bank_mask:0xf\n"
    "v_mov_b32_dpp v131, v33 row_newbcast:3 row_mask:0xf bank_mask:0xf\n"
    "v_mov_b32_dpp v135, v37 row_newbcast:3 row_mask:0xf bank_mask:0xf\n"
    "v_pk_mul_f32 v[0:1], v[0:1], v[128:129]\n"
    "v_pk_mul_f32 v[2:3], v[2:3], v[130:131]\n"
    "v_pk_mul_f32 v[4:5], v[4:5], v[132:133]\n"
    "v_pk_mul_f32 v[6:7], v[6:7], v[134:135]\n"
    "v_pk_mul_f32 v[8:9], v[8:9], v[128:129]\n"
    "v_pk_mul_f32 v[10:11], v[10:11], v[130:131]\n"
    "v_pk_mul_f32 v[12:13], v[12:13], v[132:133]\n"
    "v_pk_mul_f32 v[14:15], v[14:15], v[134:135]\n"
    "v_pk_mul_f32 v[16:17], v[16:17], v[128:129]\n"
    "v_pk_mul_f32 v[18:19], v[18:19], v[130:131]\n"
    "v_pk_mul_f32 v[20:21], v[20:21], v[132:133]\n"
    "v_pk_mul_f32 v[22:23], v[22:23], v[134:135]\n"
    "v_pk_mul_f32 v[24:25], v[24:25], v[128:129]\n"
    "v_pk_mul_f32 v[26:27], v[26:27], v[130:131]\n"
    "v_pk_mul_f32 v[28:29], v[28:29], v[132:133]\n"
    "v_pk_mul_f32 v[30:31], v[30:31], v[134:135]\n"
    "s_waitcnt lgkmcnt(0)\n"
    "s_barrier\n"
    "ds_read_b32 v136, v124\n"
    "ds_read_b32 v138, v101\n"
    "ds_read_b32 v140, v102\n"
    "ds_read_b32 v142, v103\n"
    "ds_read_b64_tr_b16 v[72:73], v108 offset:0\n"
    "ds_read_b64_tr_b16 v[74:75], v108 offset:1024\n"
    "ds_read_b64_tr_b16 v[76:77], v109 offset:0\n"
    "ds_read_b64_tr_b16 v[78:79], v109 offset:1024\n"
    "ds_read_b64_tr_b16 v[80:81], v110 offset:0\n"
    "ds_read_b64_tr_b16 v[82:83], v110 offset:1024\n"
    "ds_read_b64_tr_b16 v[84:85], v111 offset:0\n"
    "ds_read_b64_tr_b16 v[86:87], v111 offset:1024\n"
    "v_pk_mul_f32 v[40:41], v[40:41], v[128:129]\n"
    "v_pk_mul_f32 v[42:43], v[42:43], v[130:131]\n"
    "v_pk_mul_f32 v[44:45], v[44:45], v[132:133]\n"
    "v_pk_mul_f32 v[46:47], v[46:47], v[134:135]\n"
    "s_waitcnt lgkmcnt(11)\n"
    "v_pk_fma_f32 v[0:1], v[136:137], v[40:41], v[0:1] op_sel_hi:[0,1,1]\n"
    "v_pk_fma_f32 v[2:3], v[136:137], v[42:43], v[2:3] op_sel_hi:[0,1,1]\n"
    "v_pk_fma_f32 v[4:5], v[136:137], v[44:45], v[4:5] op_sel_hi:[0,1,1]\n"
    "v_pk_fma_f32 v[6:7], v[136:137], v[46:47], v[6:7] op_sel_hi:[0,1,1]\n"
    "v_cvt_pk_bf16_f32 v128, v0, v1\n"
    "v_cvt_pk_bf16_f32 v129, v2, v3\n"
    "v_cvt_pk_bf16_f32 v130, v4, v5\n"
    "v_cvt_pk_bf16_f32 v131, v6, v7\n"
    "buffer_store_dwordx4 v[128:131], v122, s[52:55], s56 offen sc1\n"
    "s_waitcnt lgkmcnt(10)\n"
    "v_pk_fma_f32 v[8:9], v[138:139], v[40:41], v[8:9] op_sel_hi:[0,1,1]\n"
    "v_pk_fma_f32 v[10:11], v[138:139], v[42:43], v[10:11] op_sel_hi:[0,1,1]\n"
    "v_pk_fma_f32 v[12:13], v[138:139], v[44:45], v[12:13] op_sel_hi:[0,1,1]\n"
    "v_pk_fma_f32 v[14:15], v[138:139], v[46:47], v[14:15] op_sel_hi:[0,1,1]\n"
    "v_cvt_pk_bf16_f32 v132, v8, v9\n"
    "v_cvt_pk_bf16_f32 v133, v10, v11\n"
    "v_cvt_pk_bf16_f32 v134, v12, v13\n"
    "v_cvt_pk_bf16_f32 v135, v14, v15\n"
    "buffer_store_dwordx4 v[132:135], v122, s[52:55], s57 offen sc1\n"
    "s_waitcnt lgkmcnt(9)\n"
    "v_pk_fma_f32 v[16:17], v[140:141], v[40:41], v[16:17] op_sel_hi:[0,1,1]\n"
    "v_pk_fma_f32 v[18:19], v[140:141], v[42:43], v[18:19] op_sel_hi:[0,1,1]\n"
    "v_pk_fma_f32 v[20:21], v[140:141], v[44:45], v[20:21] op_sel_hi:[0,1,1]\n"
    "v_pk_fma_f32 v[22:23], v[140:141], v[46:47], v[22:23] op_sel_hi:[0,1,1]\n"
    "v_cvt_pk_bf16_f32 v136, v16, v17\n"
    "v_cvt_pk_bf16_f32 v137, v18, v19\n"
    "v_cvt_pk_bf16_f32 v138, v20, v21\n"
    "v_cvt_pk_bf16_f32 v139, v22, v23\n"
    "s_waitcnt lgkmcnt(8)\n"
    "v_pk_fma_f32 v[24:25], v[142:143], v[40:41], v[24:25] op_sel_hi:[0,1,1]\n"
    "v_pk_fma_f32 v[26:27], v[142:143], v[42:43], v[26:27] op_sel_hi:[0,1,1]\n"
    "v_pk_fma_f32 v[28:29], v[142:143], v[44:45], v[28:29] op_sel_hi:[0,1,1]\n"
    "v_pk_fma_f32 v[30:31], v[142:143], v[46:47], v[30:31] op_sel_hi:[0,1,1]\n"
    "v_cvt_pk_bf16_f32 v140, v24, v25\n"
    "v_cvt_pk_bf16_f32 v141, v26, v27\n"
    "v_cvt_pk_bf16_f32 v142, v28, v29\n"
    "v_cvt_pk_bf16_f32 v143, v30, v31\n"
    "s_waitcnt lgkmcnt(4)\n"
    "v_mfma_f32_16x16x32_bf16 v[0:3], v[72:75], v[144:147], 0\n"
    "ds_read_b64_tr_b16 v[88:89], v112 offset:0\n"
    "ds_read_b64_tr_b16 v[90:91], v112 offset:1024\n"
    "v_mfma_f32_16x16x32_bf16 v[4:7], v[72:75], v[148:151], 0\n"
    "ds_read_b64_tr_b16 v[92:93], v113 offset:0\n"
    "ds_read_b64_tr_b16 v[94:95], v113 offset:1024\n"
    "v_mfma_f32_16x16x32_bf16 v[64:67], v[72:75], v[104:107], 0\n"
    "ds_read_b64_tr_b16 v[96:97], v114 offset:0\n"
    "ds_read_b64_tr_b16 v[98:99], v114 offset:1024\n"
    "v_mfma_f32_16x16x32_bf16 v[8:11], v[76:79], v[144:147], 0\n"
    "ds_read_b64_tr_b16 v[100:101], v115 offset:0\n"
    "ds_read_b64_tr_b16 v[102:103], v115 offset:1024\n"
    "v_mfma_f32_16x16x32_bf16 v[12:15], v[76:79], v[148:151], 0\n"
    "v_mfma_f32_16x16x32_bf16 v[68:71], v[76:79], v[104:107], 0\n"
    "buffer_store_dwordx4 v[136:139], v122, s[52:55], s58 offen sc1\n"
    "s_waitcnt lgkmcnt(8)\n"
    "v_mfma_f32_16x16x32_bf16 v[16:19], v[80:83], v[144:147], 0\n"
    "v_mfma_f32_16x16x32_bf16 v[20:23], v[80:83], v[148:151], 0\n"
    "v_mfma_f32_16x16x32_bf16 v[24:27], v[84:87], v[144:147], 0\n"
    "v_mfma_f32_16x16x32_bf16 v[28:31], v[84:87], v[148:151], 0\n"
    "s_waitcnt lgkmcnt(4)\n"
    "v_mfma_f32_16x16x32_bf16 v[32:35], v[88:91], v[144:147], 0\n"
    "ds_read_b64_tr_b16 v[72:73], v108 offset:8192\n"
    "ds_read_b64_tr_b16 v[74:75], v108 offset:9216\n"
    "v_mfma_f32_16x16x32_bf16 v[36:39], v[88:91], v[148:151], 0\n"
    "ds_read_b64_tr_b16 v[76:77], v109 offset:8192\n"
    "ds_read_b64_tr_b16 v[78:79], v109 offset:9216\n"
    "v_mfma_f32_16x16x32_bf16 v[40:43], v[92:95], v[144:147], 0\n"
    "ds_read_b64_tr_b16 v[80:81], v110 offset:8192\n"
    "ds_read_b64_tr_b16 v[82:83], v110 offset:9216\n"
    "v_mfma_f32_16x16x32_bf16 v[44:47], v[92:95], v[148:151], 0\n"
    "ds_read_b64_tr_b16 v[84:85], v111 offset:8192\n"
    "s_waitcnt lgkmcnt(7)\n"
    "v_mfma_f32_16x16x32_bf16 v[48:51], v[96:99], v[144:147], 0\n"
    "ds_read_b64_tr_b16 v[86:87], v111 offset:9216\n"
    "v_mfma_f32_16x16x32_bf16 v[52:55], v[96:99], v[148:151], 0\n"
    "v_mfma_f32_16x16x32_bf16 v[56:59], v[100:103], v[144:147], 0\n"
    "buffer_store_dwordx4 v[140:143], v122, s[52:55], s59 offen sc1\n"
    "v_mfma_f32_16x16x32_bf16 v[60:63], v[100:103], v[148:151], 0\n"
    "s_waitcnt lgkmcnt(4)\n"
    "v_mfma_f32_16x16x32_bf16 v[0:3], v[72:75], v[152:155], v[0:3]\n"
    "ds_read_b64_tr_b16 v[88:89], v112 offset:8192\n"
    "ds_read_b64_tr_b16 v[90:91], v112 offset:9216\n"
    "v_mfma_f32_16x16x32_bf16 v[4:7], v[72:75], v[156:159], v[4:7]\n"
    "ds_read_b64_tr_b16 v[92:93], v113 offset:8192\n"
    "ds_read_b64_tr_b16 v[94:95], v113 offset:9216\n"
    "v_mfma_f32_16x16x32_bf16 v[64:67], v[72:75], v[104:107], v[64:67]\n"
    "ds_read_b64_tr_b16 v[96:97], v114 offset:8192\n"
    "ds_read_b64_tr_b16 v[98:99], v114 offset:9216\n"
    "v_mfma_f32_16x16x32_bf16 v[8:11], v[76:79], v[152:155], v[8:11]\n"
    "ds_read_b64_tr_b16 v[100:101], v115 offset:8192\n"
    "ds_read_b64_tr_b16 v[102:103], v115 offset:9216\n"
    "v_mfma_f32_16x16x32_bf16 v[12:15], v[76:79], v[156:159], v[12:15]\n"
    "v_mfma_f32_16x16x32_bf16 v[68:71], v[76:79], v[104:107], v[68:71]\n"
    "v_mov_b32 v148, -1\n"
    "s_waitcnt lgkmcnt(8)\n"
    "v_mfma_f32_16x16x32_bf16 v[16:19], v[80:83], v[152:155], v[16:19]\n"
    "v_mfma_f32_16x16x32_bf16 v[20:23], v[80:83], v[156:159], v[20:23]\n"
    "v_mov_b32 v149, -1\n"
    "v_mfma_f32_16x16x32_bf16 v[24:27], v[84:87], v[152:155], v[24:27]\n"
    "v_mfma_f32_16x16x32_bf16 v[28:31], v[84:87], v[156:159], v[28:31]\n"
    "v_mov_b32 v150, -1\n"
    "s_waitcnt lgkmcnt(0)\n"
    "s_barrier\n"
    "v_mfma_f32_16x16x32_bf16 v[32:35], v[88:91], v[152:155], v[32:35]\n"
    "ds_read_b64_tr_b16 v[72:73], v108 offset:16384\n"
    "ds_read_b64_tr_b16 v[74:75], v108 offset:17408\n"
    "v_mfma_f32_16x16x32_bf16 v[36:39], v[88:91], v[156:159], v[36:39]\n"
    "ds_read_b64_tr_b16 v[76:77], v109 offset:16384\n"
    "ds_read_b64_tr_b16 v[78:79], v109 offset:17408\n"
    "v_mfma_f32_16x16x32_bf16 v[40:43], v[92:95], v[152:155], v[40:43]\n"
    "ds_read_b64_tr_b16 v[80:81], v110 offset:16384\n"
    "ds_read_b64_tr_b16 v[82:83], v110 offset:17408\n"
    "v_mfma_f32_16x16x32_bf16 v[44:47], v[92:95], v[156:159], v[44:47]\n"
    "ds_read_b64_tr_b16 v[84:85], v111 offset:16384\n"
    "v_mfma_f32_16x16x32_bf16 v[48:51], v[96:99], v[152:155], v[48:51]\n"
    "ds_read_b64_tr_b16 v[86:87], v111 offset:17408\n"
    "v_mfma_f32_16x16x32_bf16 v[52:55], v[96:99], v[156:159], v[52:55]\n"
    "v_mfma_f32_16x16x32_bf16 v[56:59], v[100:103], v[152:155], v[56:59]\n"
    "v_mfma_f32_16x16x32_bf16 v[60:63], v[100:103], v[156:159], v[60:63]\n"
    "s_waitcnt lgkmcnt(4)\n"
    "v_mfma_f32_16x16x32_bf16 v[0:3], v[72:75], v[160:163], v[0:3]\n"
    "ds_read_b64_tr_b16 v[88:89], v112 offset:16384\n"
    "ds_read_b64_tr_b16 v[90:91], v112 offset:17408\n"
    "v_mfma_f32_16x16x32_bf16 v[4:7], v[72:75], v[164:167], v[4:7]\n"
    "ds_read_b64_tr_b16 v[92:93], v113 offset:16384\n"
    "ds_read_b64_tr_b16 v[94:95], v113 offset:17408\n"
    "v_mfma_f32_16x16x32_bf16 v[64:67], v[72:75], v[104:107], v[64:67]\n"
    "ds_read_b64_tr_b16 v[96:97], v114 offset:16384\n"
    "ds_read_b64_tr_b16 v[98:99], v114 offset:17408\n"
    "v_mfma_f32_16x16x32_bf16 v[8:11], v[76:79], v[160:163], v[8:11]\n"
    "ds_read_b64_tr_b16 v[100:101], v115 offset:16384\n"
    "ds_read_b64_tr_b16 v[102:103], v115 offset:17408\n"
    "v_mfma_f32_16x16x32_bf16 v[12:15], v[76:79], v[164:167], v[12:15]\n"
    "v_mfma_f32_16x16x32_bf16 v[68:71], v[76:79], v[104:107], v[68:71]\n"
    "v_mov_b32 v151, -1\n"
    "s_waitcnt lgkmcnt(8)\n"
    "v_mfma_f32_16x16x32_bf16 v[16:19], v[80:83], v[160:163], v[16:19]\n"
    "v_mfma_f32_16x16x32_bf16 v[20:23], v[80:83], v[164:167], v[20:23]\n"
    "v_permlane16_swap_b32 v116, v117\n"
    "v_mfma_f32_16x16x32_bf16 v[24:27], v[84:87], v[160:163], v[24:27]\n"
    "v_mfma_f32_16x16x32_bf16 v[28:31], v[84:87], v[164:167], v[28:31]\n"
    "v_permlane16_swap_b32 v118, v119\n"
    "s_waitcnt lgkmcnt(4)\n"
    "v_mfma_f32_16x16x32_bf16 v[32:35], v[88:91], v[160:163], v[32:35]\n"
    "ds_read_b64_tr_b16 v[72:73], v108 offset:24576\n"
    "ds_read_b64_tr_b16 v[74:75], v108 offset:25600\n"
    "v_mfma_f32_16x16x32_bf16 v[36:39], v[88:91], v[164:167], v[36:39]\n"
    "ds_read_b64_tr_b16 v[76:77], v109 offset:24576\n"
    "ds_read_b64_tr_b16 v[78:79], v109 offset:25600\n"
    "v_mfma_f32_16x16x32_bf16 v[40:43], v[92:95], v[160:163], v[40:43]\n"
    "ds_read_b64_tr_b16 v[80:81], v110 offset:24576\n"
    "ds_read_b64_tr_b16 v[82:83], v110 offset:25600\n"
    "v_mfma_f32_16x16x32_bf16 v[44:47], v[92:95], v[164:167], v[44:47]\n"
    "ds_read_b64_tr_b16 v[84:85], v111 offset:24576\n"
    "s_waitcnt lgkmcnt(7)\n"
    "v_mfma_f32_16x16x32_bf16 v[48:51], v[96:99], v[160:163], v[48:51]\n"
    "ds_read_b64_tr_b16 v[86:87], v111 offset:25600\n"
    "v_mfma_f32_16x16x32_bf16 v[52:55], v[96:99], v[164:167], v[52:55]\n"
    "v_mfma_f32_16x16x32_bf16 v[56:59], v[100:103], v[160:163], v[56:59]\n"
    "v_mfma_f32_16x16x32_bf16 v[60:63], v[100:103], v[164:167], v[60:63]\n"
    "s_waitcnt lgkmcnt(4)\n"
    "v_mfma_f32_16x16x32_bf16 v[0:3], v[72:75], v[168:171], v[0:3]\n"
    "ds_read_b64_tr_b16 v[88:89], v112 offset:24576\n"
    "ds_read_b64_tr_b16 v[90:91], v112 offset:25600\n"
    "v_mfma_f32_16x16x32_bf16 v[4:7], v[72:75], v[172:175], v[4:7]\n"
    "ds_read_b64_tr_b16 v[92:93], v113 offset:24576\n"
    "ds_read_b64_tr_b16 v[94:95], v113 offset:25600\n"
    "v_mfma_f32_16x16x32_bf16 v[64:67], v[72:75], v[104:107], v[64:67]\n"
    "ds_read_b64_tr_b16 v[96:97], v114 offset:24576\n"
    "ds_read_b64_tr_b16 v[98:99], v114 offset:25600\n"
    "v_mfma_f32_16x16x32_bf16 v[8:11], v[76:79], v[168:171], v[8:11]\n"
    "ds_read_b64_tr_b16 v[100:101], v115 offset:24576\n"
    "ds_read_b64_tr_b16 v[102:103], v115 offset:25600\n"
    "v_mfma_f32_16x16x32_bf16 v[12:15], v[76:79], v[172:175], v[12:15]\n"
    "v_mfma_f32_16x16x32_bf16 v[68:71], v[76:79], v[104:107], v[68:71]\n"
    "v_cndmask_b32 v116, v116, v117, s[88:89]\n"
    "s_waitcnt lgkmcnt(8)\n"
    "v_mfma_f32_16x16x32_bf16 v[16:19], v[80:83], v[168:171], v[16:19]\n"
    "v_mfma_f32_16x16x32_bf16 v[20:23], v[80:83], v[172:175], v[20:23]\n"
    "v_cndmask_b32 v118, v118, v119, s[88:89]\n"
    "v_mfma_f32_16x16x32_bf16 v[24:27], v[84:87], v[168:171], v[24:27]\n"
    "v_mfma_f32_16x16x32_bf16 v[28:31], v[84:87], v[172:175], v[28:31]\n"
    "v_permlane32_swap_b32 v116, v118\n"
    "s_waitcnt lgkmcnt(0)\n"
    "s_waitcnt vmcnt(0)\n"
    "s_barrier\n"
    "v_mfma_f32_16x16x32_bf16 v[32:35], v[88:91], v[168:171], v[32:35]\n"
    "ds_read_b64_tr_b16 v[72:73], v108 offset:32768\n"
    "ds_read_b64_tr_b16 v[74:75], v108 offset:33792\n"
    "v_mfma_f32_16x16x32_bf16 v[36:39], v[88:91], v[172:175], v[36:39]\n"
    "ds_read_b64_tr_b16 v[76:77], v109 offset:32768\n"
    "ds_read_b64_tr_b16 v[78:79], v109 offset:33792\n"
    "v_mfma_f32_16x16x32_bf16 v[40:43], v[92:95], v[168:171], v[40:43]\n"
    "ds_read_b64_tr_b16 v[80:81], v110 offset:32768\n"
    "ds_read_b64_tr_b16 v[82:83], v110 offset:33792\n"
    "v_mfma_f32_16x16x32_bf16 v[44:47], v[92:95], v[172:175], v[44:47]\n"
    "ds_read_b64_tr_b16 v[84:85], v111 offset:32768\n"
    "v_mfma_f32_16x16x32_bf16 v[48:51], v[96:99], v[168:171], v[48:51]\n"
    "ds_read_b64_tr_b16 v[86:87], v111 offset:33792\n"
    "v_mfma_f32_16x16x32_bf16 v[52:55], v[96:99], v[172:175], v[52:55]\n"
    "v_mfma_f32_16x16x32_bf16 v[56:59], v[100:103], v[168:171], v[56:59]\n"
    "v_mfma_f32_16x16x32_bf16 v[60:63], v[100:103], v[172:175], v[60:63]\n"
    "s_waitcnt lgkmcnt(4)\n"
    "v_mfma_f32_16x16x32_bf16 v[0:3], v[72:75], v[176:179], v[0:3]\n"
    "ds_read_b64_tr_b16 v[88:89], v112 offset:32768\n"
    "ds_read_b64_tr_b16 v[90:91], v112 offset:33792\n"
    "v_mfma_f32_16x16x32_bf16 v[4:7], v[72:75], v[180:183], v[4:7]\n"
    "ds_read_b64_tr_b16 v[92:93], v113 offset:32768\n"
    "ds_read_b64_tr_b16 v[94:95], v113 offset:33792\n"
    "v_mfma_f32_16x16x32_bf16 v[64:67], v[72:75], v[104:107], v[64:67]\n"
    "ds_read_b64_tr_b16 v[96:97], v114 offset:32768\n"
    "ds_read_b64_tr_b16 v[98:99], v114 offset:33792\n"
    "v_mfma_f32_16x16x32_bf16 v[8:11], v[76:79], v[176:179], v[8:11]\n"
    "ds_read_b64_tr_b16 v[100:101], v115 offset:32768\n"
    "ds_read_b64_tr_b16 v[102:103], v115 offset:33792\n"
    "v_mfma_f32_16x16x32_bf16 v[12:15], v[76:79], v[180:183], v[12:15]\n"
    "v_mfma_f32_16x16x32_bf16 v[68:71], v[76:79], v[104:107], v[68:71]\n"
    "v_cndmask_b32 v116, v116, v118, s[90:91]\n"
    "s_waitcnt lgkmcnt(8)\n"
    "v_mfma_f32_16x16x32_bf16 v[16:19], v[80:83], v[176:179], v[16:19]\n"
    "v_mfma_f32_16x16x32_bf16 v[20:23], v[80:83], v[180:183], v[20:23]\n"
    "v_mov_b32 v118, v116\n"
    "v_mfma_f32_16x16x32_bf16 v[24:27], v[84:87], v[176:179], v[24:27]\n"
    "v_mfma_f32_16x16x32_bf16 v[28:31], v[84:87], v[180:183], v[28:31]\n"
    "v_permlane32_swap_b32 v116, v118\n"
    "s_waitcnt lgkmcnt(4)\n"
    "v_mfma_f32_16x16x32_bf16 v[32:35], v[88:91], v[176:179], v[32:35]\n"
    "ds_read_b64_tr_b16 v[72:73], v108 offset:40960\n"
    "ds_read_b64_tr_b16 v[74:75], v108 offset:41984\n"
    "v_mfma_f32_16x16x32_bf16 v[36:39], v[88:91], v[180:183], v[36:39]\n"
    "ds_read_b64_tr_b16 v[76:77], v109 offset:40960\n"
    "ds_read_b64_tr_b16 v[78:79], v109 offset:41984\n"
    "v_mfma_f32_16x16x32_bf16 v[40:43], v[92:95], v[176:179], v[40:43]\n"
    "ds_read_b64_tr_b16 v[80:81], v110 offset:40960\n"
    "ds_read_b64_tr_b16 v[82:83], v110 offset:41984\n"
    "v_mfma_f32_16x16x32_bf16 v[44:47], v[92:95], v[180:183], v[44:47]\n"
    "ds_read_b64_tr_b16 v[84:85], v111 offset:40960\n"
    "s_waitcnt lgkmcnt(7)\n"
    "v_mfma_f32_16x16x32_bf16 v[48:51], v[96:99], v[176:179], v[48:51]\n"
    "ds_read_b64_tr_b16 v[86:87], v111 offset:41984\n"
    "v_mfma_f32_16x16x32_bf16 v[52:55], v[96:99], v[180:183], v[52:55]\n"
    "v_mfma_f32_16x16x32_bf16 v[56:59], v[100:103], v[176:179], v[56:59]\n"
    "v_mfma_f32_16x16x32_bf16 v[60:63], v[100:103], v[180:183], v[60:63]\n"
    "s_waitcnt lgkmcnt(4)\n"
    "v_mfma_f32_16x16x32_bf16 v[0:3], v[72:75], v[184:187], v[0:3]\n"
    "ds_read_b64_tr_b16 v[88:89], v112 offset:40960\n"
    "ds_read_b64_tr_b16 v[90:91], v112 offset:41984\n"
    "v_mfma_f32_16x16x32_bf16 v[4:7], v[72:75], v[188:191], v[4:7]\n"
    "ds_read_b64_tr_b16 v[92:93], v113 offset:40960\n"
    "ds_read_b64_tr_b16 v[94:95], v113 offset:41984\n"
    "v_mfma_f32_16x16x32_bf16 v[64:67], v[72:75], v[104:107], v[64:67]\n"
    "ds_read_b64_tr_b16 v[96:97], v114 offset:40960\n"
    "ds_read_b64_tr_b16 v[98:99], v114 offset:41984\n"
    "v_mfma_f32_16x16x32_bf16 v[8:11], v[76:79], v[184:187], v[8:11]\n"
    "ds_read_b64_tr_b16 v[100:101], v115 offset:40960\n"
    "ds_read_b64_tr_b16 v[102:103], v115 offset:41984\n"
    "v_mfma_f32_16x16x32_bf16 v[12:15], v[76:79], v[188:191], v[12:15]\n"
    "v_mfma_f32_16x16x32_bf16 v[68:71], v[76:79], v[104:107], v[68:71]\n"
    "v_mov_b32 v117, v116\n"
    "s_waitcnt lgkmcnt(8)\n"
    "v_mfma_f32_16x16x32_bf16 v[16:19], v[80:83], v[184:187], v[16:19]\n"
    "v_mov_b32 v119, v118\n"
    "v_mfma_f32_16x16x32_bf16 v[20:23], v[80:83], v[188:191], v[20:23]\n"
    "v_permlane16_swap_b32 v116, v117\n"
    "v_add_u32 v108, 49152, v108\n"
    "v_mfma_f32_16x16x32_bf16 v[24:27], v[84:87], v[184:187], v[24:27]\n"
    "v_add_u32 v109, 49152, v109\n"
    "v_mfma_f32_16x16x32_bf16 v[28:31], v[84:87], v[188:191], v[28:31]\n"
    "v_add_u32 v110, 49152, v110\n"
    "v_add_u32 v111, 49152, v111\n"
    "s_waitcnt lgkmcnt(0)\n"
    "s_barrier\n"
    "v_mfma_f32_16x16x32_bf16 v[32:35], v[88:91], v[184:187], v[32:35]\n"
    "ds_read_b64_tr_b16 v[72:73], v108 offset:49152\n"
    "ds_read_b64_tr_b16 v[74:75], v108 offset:50176\n"
    "v_mfma_f32_16x16x32_bf16 v[36:39], v[88:91], v[188:191], v[36:39]\n"
    "ds_read_b64_tr_b16 v[76:77], v109 offset:49152\n"
    "ds_read_b64_tr_b16 v[78:79], v109 offset:50176\n"
    "v_mfma_f32_16x16x32_bf16 v[40:43], v[92:95], v[184:187], v[40:43]\n"
    "ds_read_b64_tr_b16 v[80:81], v110 offset:49152\n"
    "ds_read_b64_tr_b16 v[82:83], v110 offset:50176\n"
    "v_mfma_f32_16x16x32_bf16 v[44:47], v[92:95], v[188:191], v[44:47]\n"
    "ds_read_b64_tr_b16 v[84:85], v111 offset:49152\n"
    "v_mfma_f32_16x16x32_bf16 v[48:51], v[96:99], v[184:187], v[48:51]\n"
    "ds_read_b64_tr_b16 v[86:87], v111 offset:50176\n"
    "v_add_u32 v112, 49152, v112\n"
    "v_mfma_f32_16x16x32_bf16 v[52:55], v[96:99], v[188:191], v[52:55]\n"
    "v_add_u32 v113, 49152, v113\n"
    "v_mfma_f32_16x16x32_bf16 v[56:59], v[100:103], v[184:187], v[56:59]\n"
    "v_add_u32 v114, 49152, v114\n"
    "v_mfma_f32_16x16x32_bf16 v[60:63], v[100:103], v[188:191], v[60:63]\n"
    "v_add_u32 v115, 49152, v115\n"
    "s_waitcnt lgkmcnt(4)\n"
    "v_mfma_f32_16x16x32_bf16 v[0:3], v[72:75], v[192:195], v[0:3]\n"
    "ds_read_b64_tr_b16 v[88:89], v112 offset:49152\n"
    "ds_read_b64_tr_b16 v[90:91], v112 offset:50176\n"
    "v_mfma_f32_16x16x32_bf16 v[4:7], v[72:75], v[196:199], v[4:7]\n"
    "ds_read_b64_tr_b16 v[92:93], v113 offset:49152\n"
    "ds_read_b64_tr_b16 v[94:95], v113 offset:50176\n"
    "v_mfma_f32_16x16x32_bf16 v[64:67], v[72:75], v[104:107], v[64:67]\n"
    "ds_read_b64_tr_b16 v[96:97], v114 offset:49152\n"
    "ds_read_b64_tr_b16 v[98:99], v114 offset:50176\n"
    "v_mfma_f32_16x16x32_bf16 v[8:11], v[76:79], v[192:195], v[8:11]\n"
    "ds_read_b64_tr_b16 v[100:101], v115 offset:49152\n"
    "ds_read_b64_tr_b16 v[102:103], v115 offset:50176\n"
    "v_mfma_f32_16x16x32_bf16 v[12:15], v[76:79], v[196:199], v[12:15]\n"
    "v_mfma_f32_16x16x32_bf16 v[68:71], v[76:79], v[104:107], v[68:71]\n"
    "v_permlane16_swap_b32 v118, v119\n"
    "s_waitcnt lgkmcnt(8)\n"
    "v_mfma_f32_16x16x32_bf16 v[16:19], v[80:83], v[192:195], v[16:19]\n"
    "v_mfma_f32_16x16x32_bf16 v[20:23], v[80:83], v[196:199], v[20:23]\n"
    "v_xor_b32 v145, 128, v125\n"
    "v_mfma_f32_16x16x32_bf16 v[24:27], v[84:87], v[192:195], v[24:27]\n"
    "v_mfma_f32_16x16x32_bf16 v[28:31], v[84:87], v[196:199], v[28:31]\n"
    "v_xor_b32 v146, 256, v125\n"
    "s_waitcnt lgkmcnt(4)\n"
    "v_mfma_f32_16x16x32_bf16 v[32:35], v[88:91], v[192:195], v[32:35]\n"
    "ds_read_b64_tr_b16 v[72:73], v108 offset:57344\n"
    "ds_read_b64_tr_b16 v[74:75], v108 offset:58368\n"
    "v_mfma_f32_16x16x32_bf16 v[36:39], v[88:91], v[196:199], v[36:39]\n"
    "ds_read_b64_tr_b16 v[76:77], v109 offset:57344\n"
    "ds_read_b64_tr_b16 v[78:79], v109 offset:58368\n"
    "v_mfma_f32_16x16x32_bf16 v[40:43], v[92:95], v[192:195], v[40:43]\n"
    "ds_read_b64_tr_b16 v[80:81], v110 offset:57344\n"
    "ds_read_b64_tr_b16 v[82:83], v110 offset:58368\n"
    "v_mfma_f32_16x16x32_bf16 v[44:47], v[92:95], v[196:199], v[44:47]\n"
    "ds_read_b64_tr_b16 v[84:85], v111 offset:57344\n"
    "s_waitcnt lgkmcnt(7)\n"
    "v_mfma_f32_16x16x32_bf16 v[48:51], v[96:99], v[192:195], v[48:51]\n"
    "ds_read_b64_tr_b16 v[86:87], v111 offset:58368\n"
    "v_mfma_f32_16x16x32_bf16 v[52:55], v[96:99], v[196:199], v[52:55]\n"
    "v_mfma_f32_16x16x32_bf16 v[56:59], v[100:103], v[192:195], v[56:59]\n"
    "v_mfma_f32_16x16x32_bf16 v[60:63], v[100:103], v[196:199], v[60:63]\n"
    "s_waitcnt lgkmcnt(4)\n"
    "v_mfma_f32_16x16x32_bf16 v[0:3], v[72:75], v[200:203], v[0:3]\n"
    "ds_read_b64_tr_b16 v[88:89], v112 offset:57344\n"
    "ds_read_b64_tr_b16 v[90:91], v112 offset:58368\n"
    "v_mfma_f32_16x16x32_bf16 v[4:7], v[72:75], v[204:207], v[4:7]\n"
    "ds_read_b64_tr_b16 v[92:93], v113 offset:57344\n"
    "ds_read_b64_tr_b16 v[94:95], v113 offset:58368\n"
    "v_mfma_f32_16x16x32_bf16 v[64:67], v[72:75], v[104:107], v[64:67]\n"
    "ds_read_b64_tr_b16 v[96:97], v114 offset:57344\n"
    "ds_read_b64_tr_b16 v[98:99], v114 offset:58368\n"
    "v_mfma_f32_16x16x32_bf16 v[8:11], v[76:79], v[200:203], v[8:11]\n"
    "ds_read_b64_tr_b16 v[100:101], v115 offset:57344\n"
    "ds_read_b64_tr_b16 v[102:103], v115 offset:58368\n"
    "v_mfma_f32_16x16x32_bf16 v[12:15], v[76:79], v[204:207], v[12:15]\n"
    "v_mfma_f32_16x16x32_bf16 v[68:71], v[76:79], v[104:107], v[68:71]\n"
    "v_xor_b32 v147, 384, v125\n"
    "s_waitcnt lgkmcnt(8)\n"
    "v_mfma_f32_16x16x32_bf16 v[16:19], v[80:83], v[200:203], v[16:19]\n"
    "v_mfma_f32_16x16x32_bf16 v[20:23], v[80:83], v[204:207], v[20:23]\n"
    "v_mfma_f32_16x16x32_bf16 v[24:27], v[84:87], v[200:203], v[24:27]\n"
    "v_mfma_f32_16x16x32_bf16 v[28:31], v[84:87], v[204:207], v[28:31]\n"
    "s_waitcnt lgkmcnt(0)\n"
    "s_barrier\n"
    "v_mfma_f32_16x16x32_bf16 v[32:35], v[88:91], v[200:203], v[32:35]\n"
    "ds_read_b64_tr_b16 v[72:73], v108 offset:32768\n"
    "ds_read_b64_tr_b16 v[74:75], v108 offset:33792\n"
    "v_mfma_f32_16x16x32_bf16 v[36:39], v[88:91], v[204:207], v[36:39]\n"
    "ds_read_b64_tr_b16 v[76:77], v109 offset:32768\n"
    "ds_read_b64_tr_b16 v[78:79], v109 offset:33792\n"
    "v_mfma_f32_16x16x32_bf16 v[40:43], v[92:95], v[200:203], v[40:43]\n"
    "ds_read_b64_tr_b16 v[80:81], v110 offset:32768\n"
    "ds_read_b64_tr_b16 v[82:83], v110 offset:33792\n"
    "v_mfma_f32_16x16x32_bf16 v[44:47], v[92:95], v[204:207], v[44:47]\n"
    "ds_read_b64_tr_b16 v[84:85], v111 offset:32768\n"
    "v_mfma_f32_16x16x32_bf16 v[48:51], v[96:99], v[200:203], v[48:51]\n"
    "ds_read_b64_tr_b16 v[86:87], v111 offset:33792\n"
    "v_mfma_f32_16x16x32_bf16 v[52:55], v[96:99], v[204:207], v[52:55]\n"
    "v_mfma_f32_16x16x32_bf16 v[56:59], v[100:103], v[200:203], v[56:59]\n"
    "v_mfma_f32_16x16x32_bf16 v[60:63], v[100:103], v[204:207], v[60:63]\n"
    "s_waitcnt lgkmcnt(4)\n"
    "v_mfma_f32_16x16x32_bf16 v[0:3], v[72:75], v[208:211], v[0:3]\n"
    "ds_read_b64_tr_b16 v[88:89], v112 offset:32768\n"
    "ds_read_b64_tr_b16 v[90:91], v112 offset:33792\n"
    "v_mfma_f32_16x16x32_bf16 v[4:7], v[72:75], v[212:215], v[4:7]\n"
    "ds_read_b64_tr_b16 v[92:93], v113 offset:32768\n"
    "ds_read_b64_tr_b16 v[94:95], v113 offset:33792\n"
    "v_mfma_f32_16x16x32_bf16 v[64:67], v[72:75], v[104:107], v[64:67]\n"
    "ds_read_b64_tr_b16 v[96:97], v114 offset:32768\n"
    "ds_read_b64_tr_b16 v[98:99], v114 offset:33792\n"
    "v_mfma_f32_16x16x32_bf16 v[8:11], v[76:79], v[208:211], v[8:11]\n"
    "ds_read_b64_tr_b16 v[100:101], v115 offset:32768\n"
    "ds_read_b64_tr_b16 v[102:103], v115 offset:33792\n"
    "v_mfma_f32_16x16x32_bf16 v[12:15], v[76:79], v[212:215], v[12:15]\n"
    "v_mfma_f32_16x16x32_bf16 v[68:71], v[76:79], v[104:107], v[68:71]\n"
    "s_waitcnt lgkmcnt(8)\n"
    "v_mfma_f32_16x16x32_bf16 v[16:19], v[80:83], v[208:211], v[16:19]\n"
    "v_mfma_f32_16x16x32_bf16 v[20:23], v[80:83], v[212:215], v[20:23]\n"
    "v_mfma_f32_16x16x32_bf16 v[24:27], v[84:87], v[208:211], v[24:27]\n"
    "v_mfma_f32_16x16x32_bf16 v[28:31], v[84:87], v[212:215], v[28:31]\n"
    "s_waitcnt lgkmcnt(4)\n"
    "v_mfma_f32_16x16x32_bf16 v[32:35], v[88:91], v[208:211], v[32:35]\n"
    "ds_read_b64_tr_b16 v[72:73], v108 offset:40960\n"
    "ds_read_b64_tr_b16 v[74:75], v108 offset:41984\n"
    "v_mfma_f32_16x16x32_bf16 v[36:39], v[88:91], v[212:215], v[36:39]\n"
    "ds_read_b64_tr_b16 v[76:77], v109 offset:40960\n"
    "ds_read_b64_tr_b16 v[78:79], v109 offset:41984\n"
    "v_mfma_f32_16x16x32_bf16 v[40:43], v[92:95], v[208:211], v[40:43]\n"
    "ds_read_b64_tr_b16 v[80:81], v110 offset:40960\n"
    "ds_read_b64_tr_b16 v[82:83], v110 offset:41984\n"
    "v_mfma_f32_16x16x32_bf16 v[44:47], v[92:95], v[212:215], v[44:47]\n"
    "ds_read_b64_tr_b16 v[84:85], v111 offset:40960\n"
    "s_waitcnt lgkmcnt(7)\n"
    "v_mfma_f32_16x16x32_bf16 v[48:51], v[96:99], v[208:211], v[48:51]\n"
    "ds_read_b64_tr_b16 v[86:87], v111 offset:41984\n"
    "v_mfma_f32_16x16x32_bf16 v[52:55], v[96:99], v[212:215], v[52:55]\n"
    "v_mfma_f32_16x16x32_bf16 v[56:59], v[100:103], v[208:211], v[56:59]\n"
    "v_mfma_f32_16x16x32_bf16 v[60:63], v[100:103], v[212:215], v[60:63]\n"
    "s_waitcnt lgkmcnt(4)\n"
    "v_mfma_f32_16x16x32_bf16 v[0:3], v[72:75], v[216:219], v[0:3]\n"
    "ds_read_b64_tr_b16 v[88:89], v112 offset:40960\n"
    "ds_read_b64_tr_b16 v[90:91], v112 offset:41984\n"
    "v_mfma_f32_16x16x32_bf16 v[4:7], v[72:75], v[220:223], v[4:7]\n"
    "ds_read_b64_tr_b16 v[92:93], v113 offset:40960\n"
    "ds_read_b64_tr_b16 v[94:95], v113 offset:41984\n"
    "v_mfma_f32_16x16x32_bf16 v[64:67], v[72:75], v[104:107], v[64:67]\n"
    "ds_read_b64_tr_b16 v[96:97], v114 offset:40960\n"
    "ds_read_b64_tr_b16 v[98:99], v114 offset:41984\n"
    "v_mfma_f32_16x16x32_bf16 v[8:11], v[76:79], v[216:219], v[8:11]\n"
    "ds_read_b64_tr_b16 v[100:101], v115 offset:40960\n"
    "ds_read_b64_tr_b16 v[102:103], v115 offset:41984\n"
    "v_mfma_f32_16x16x32_bf16 v[12:15], v[76:79], v[220:223], v[12:15]\n"
    "v_mfma_f32_16x16x32_bf16 v[68:71], v[76:79], v[104:107], v[68:71]\n"
    "s_waitcnt lgkmcnt(8)\n"
    "v_mfma_f32_16x16x32_bf16 v[16:19], v[80:83], v[216:219], v[16:19]\n"
    "v_mfma_f32_16x16x32_bf16 v[20:23], v[80:83], v[220:223], v[20:23]\n"
    "v_mfma_f32_16x16x32_bf16 v[24:27], v[84:87], v[216:219], v[24:27]\n"
    "v_mfma_f32_16x16x32_bf16 v[28:31], v[84:87], v[220:223], v[28:31]\n"
    "s_waitcnt lgkmcnt(0)\n"
    "s_barrier\n"
    "v_mfma_f32_16x16x32_bf16 v[32:35], v[88:91], v[216:219], v[32:35]\n"
    "ds_read_b64_tr_b16 v[72:73], v108 offset:16384\n"
    "ds_read_b64_tr_b16 v[74:75], v108 offset:17408\n"
    "v_mfma_f32_16x16x32_bf16 v[36:39], v[88:91], v[220:223], v[36:39]\n"
    "ds_read_b64_tr_b16 v[76:77], v109 offset:16384\n"
    "ds_read_b64_tr_b16 v[78:79], v109 offset:17408\n"
    "v_mfma_f32_16x16x32_bf16 v[40:43], v[92:95], v[216:219], v[40:43]\n"
    "ds_read_b64_tr_b16 v[80:81], v110 offset:16384\n"
    "ds_read_b64_tr_b16 v[82:83], v110 offset:17408\n"
    "v_mfma_f32_16x16x32_bf16 v[44:47], v[92:95], v[220:223], v[44:47]\n"
    "ds_read_b64_tr_b16 v[84:85], v111 offset:16384\n"
    "v_mfma_f32_16x16x32_bf16 v[48:51], v[96:99], v[216:219], v[48:51]\n"
    "ds_read_b64_tr_b16 v[86:87], v111 offset:17408\n"
    "v_mfma_f32_16x16x32_bf16 v[52:55], v[96:99], v[220:223], v[52:55]\n"
    "v_mfma_f32_16x16x32_bf16 v[56:59], v[100:103], v[216:219], v[56:59]\n"
    "v_mfma_f32_16x16x32_bf16 v[60:63], v[100:103], v[220:223], v[60:63]\n"
    "s_waitcnt lgkmcnt(4)\n"
    "v_mfma_f32_16x16x32_bf16 v[0:3], v[72:75], v[224:227], v[0:3]\n"
    "ds_read_b64_tr_b16 v[88:89], v112 offset:16384\n"
    "ds_read_b64_tr_b16 v[90:91], v112 offset:17408\n"
    "v_mfma_f32_16x16x32_bf16 v[4:7], v[72:75], v[228:231], v[4:7]\n"
    "ds_read_b64_tr_b16 v[92:93], v113 offset:16384\n"
    "ds_read_b64_tr_b16 v[94:95], v113 offset:17408\n"
    "v_mfma_f32_16x16x32_bf16 v[64:67], v[72:75], v[104:107], v[64:67]\n"
    "ds_read_b64_tr_b16 v[96:97], v114 offset:16384\n"
    "ds_read_b64_tr_b16 v[98:99], v114 offset:17408\n"
    "v_mfma_f32_16x16x32_bf16 v[8:11], v[76:79], v[224:227], v[8:11]\n"
    "ds_read_b64_tr_b16 v[100:101], v115 offset:16384\n"
    "ds_read_b64_tr_b16 v[102:103], v115 offset:17408\n"
    "v_mfma_f32_16x16x32_bf16 v[12:15], v[76:79], v[228:231], v[12:15]\n"
    "v_mfma_f32_16x16x32_bf16 v[68:71], v[76:79], v[104:107], v[68:71]\n"
    "s_waitcnt lgkmcnt(8)\n"
    "v_mfma_f32_16x16x32_bf16 v[16:19], v[80:83], v[224:227], v[16:19]\n"
    "v_mfma_f32_16x16x32_bf16 v[20:23], v[80:83], v[228:231], v[20:23]\n"
    "v_mfma_f32_16x16x32_bf16 v[24:27], v[84:87], v[224:227], v[24:27]\n"
    "v_mfma_f32_16x16x32_bf16 v[28:31], v[84:87], v[228:231], v[28:31]\n"
    "s_waitcnt lgkmcnt(4)\n"
    "v_mfma_f32_16x16x32_bf16 v[32:35], v[88:91], v[224:227], v[32:35]\n"
    "ds_read_b64_tr_b16 v[72:73], v108 offset:24576\n"
    "ds_read_b64_tr_b16 v[74:75], v108 offset:25600\n"
    "v_mfma_f32_16x16x32_bf16 v[36:39], v[88:91], v[228:231], v[36:39]\n"
    "ds_read_b64_tr_b16 v[76:77], v109 offset:24576\n"
    "ds_read_b64_tr_b16 v[78:79], v109 offset:25600\n"
    "v_mfma_f32_16x16x32_bf16 v[40:43], v[92:95], v[224:227], v[40:43]\n"
    "ds_read_b64_tr_b16 v[80:81], v110 offset:24576\n"
    "ds_read_b64_tr_b16 v[82:83], v110 offset:25600\n"
    "v_mfma_f32_16x16x32_bf16 v[44:47], v[92:95], v[228:231], v[44:47]\n"
    "ds_read_b64_tr_b16 v[84:85], v111 offset:24576\n"
    "s_waitcnt lgkmcnt(7)\n"
    "v_mfma_f32_16x16x32_bf16 v[48:51], v[96:99], v[224:227], v[48:51]\n"
    "ds_read_b64_tr_b16 v[86:87], v111 offset:25600\n"
    "v_mfma_f32_16x16x32_bf16 v[52:55], v[96:99], v[228:231], v[52:55]\n"
    "v_mfma_f32_16x16x32_bf16 v[56:59], v[100:103], v[224:227], v[56:59]\n"
    "v_mfma_f32_16x16x32_bf16 v[60:63], v[100:103], v[228:231], v[60:63]\n"
    "s_waitcnt lgkmcnt(4)\n"
    "v_mfma_f32_16x16x32_bf16 v[0:3], v[72:75], v[232:235], v[0:3]\n"
    "ds_read_b64_tr_b16 v[88:89], v112 offset:24576\n"
    "ds_read_b64_tr_b16 v[90:91], v112 offset:25600\n"
    "v_mfma_f32_16x16x32_bf16 v[4:7], v[72:75], v[236:239], v[4:7]\n"
    "ds_read_b64_tr_b16 v[92:93], v113 offset:24576\n"
    "ds_read_b64_tr_b16 v[94:95], v113 offset:25600\n"
    "v_mfma_f32_16x16x32_bf16 v[64:67], v[72:75], v[104:107], v[64:67]\n"
    "ds_read_b64_tr_b16 v[96:97], v114 offset:24576\n"
    "ds_read_b64_tr_b16 v[98:99], v114 offset:25600\n"
    "v_mfma_f32_16x16x32_bf16 v[8:11], v[76:79], v[232:235], v[8:11]\n"
    "ds_read_b64_tr_b16 v[100:101], v115 offset:24576\n"
    "ds_read_b64_tr_b16 v[102:103], v115 offset:25600\n"
    "v_mfma_f32_16x16x32_bf16 v[12:15], v[76:79], v[236:239], v[12:15]\n"
    "v_mfma_f32_16x16x32_bf16 v[68:71], v[76:79], v[104:107], v[68:71]\n"
    "s_waitcnt lgkmcnt(8)\n"
    "v_mfma_f32_16x16x32_bf16 v[16:19], v[80:83], v[232:235], v[16:19]\n"
    "v_mfma_f32_16x16x32_bf16 v[20:23], v[80:83], v[236:239], v[20:23]\n"
    "v_mfma_f32_16x16x32_bf16 v[24:27], v[84:87], v[232:235], v[24:27]\n"
    "v_mfma_f32_16x16x32_bf16 v[28:31], v[84:87], v[236:239], v[28:31]\n"
    "s_waitcnt lgkmcnt(0)\n"
    "s_barrier\n"
    "v_mfma_f32_16x16x32_bf16 v[32:35], v[88:91], v[232:235], v[32:35]\n"
    "ds_read_b64_tr_b16 v[72:73], v108 offset:0\n"
    "ds_read_b64_tr_b16 v[74:75], v108 offset:1024\n"
    "v_mfma_f32_16x16x32_bf16 v[36:39], v[88:91], v[236:239], v[36:39]\n"
    "ds_read_b64_tr_b16 v[76:77], v109 offset:0\n"
    "ds_read_b64_tr_b16 v[78:79], v109 offset:1024\n"
    "v_mfma_f32_16x16x32_bf16 v[40:43], v[92:95], v[232:235], v[40:43]\n"
    "ds_read_b64_tr_b16 v[80:81], v110 offset:0\n"
    "ds_read_b64_tr_b16 v[82:83], v110 offset:1024\n"
    "v_mfma_f32_16x16x32_bf16 v[44:47], v[92:95], v[236:239], v[44:47]\n"
    "ds_read_b64_tr_b16 v[84:85], v111 offset:0\n"
    "v_mfma_f32_16x16x32_bf16 v[48:51], v[96:99], v[232:235], v[48:51]\n"
    "ds_read_b64_tr_b16 v[86:87], v111 offset:1024\n"
    "v_mfma_f32_16x16x32_bf16 v[52:55], v[96:99], v[236:239], v[52:55]\n"
    "v_mfma_f32_16x16x32_bf16 v[56:59], v[100:103], v[232:235], v[56:59]\n"
    "v_mfma_f32_16x16x32_bf16 v[60:63], v[100:103], v[236:239], v[60:63]\n"
    "s_waitcnt lgkmcnt(4)\n"
    "v_mfma_f32_16x16x32_bf16 v[0:3], v[72:75], v[240:243], v[0:3]\n"
    "ds_read_b64_tr_b16 v[88:89], v112 offset:0\n"
    "ds_read_b64_tr_b16 v[90:91], v112 offset:1024\n"
    "v_mfma_f32_16x16x32_bf16 v[4:7], v[72:75], v[244:247], v[4:7]\n"
    "ds_read_b64_tr_b16 v[92:93], v113 offset:0\n"
    "ds_read_b64_tr_b16 v[94:95], v113 offset:1024\n"
    "v_mfma_f32_16x16x32_bf16 v[64:67], v[72:75], v[104:107], v[64:67]\n"
    "ds_read_b64_tr_b16 v[96:97], v114 offset:0\n"
    "ds_read_b64_tr_b16 v[98:99], v114 offset:1024\n"
    "v_mfma_f32_16x16x32_bf16 v[8:11], v[76:79], v[240:243], v[8:11]\n"
    "ds_read_b64_tr_b16 v[100:101], v115 offset:0\n"
    "ds_read_b64_tr_b16 v[102:103], v115 offset:1024\n"
    "v_mfma_f32_16x16x32_bf16 v[12:15], v[76:79], v[244:247], v[12:15]\n"
    "v_mfma_f32_16x16x32_bf16 v[68:71], v[76:79], v[104:107], v[68:71]\n"
    "s_waitcnt lgkmcnt(8)\n"
    "v_mfma_f32_16x16x32_bf16 v[16:19], v[80:83], v[240:243], v[16:19]\n"
    "v_mfma_f32_16x16x32_bf16 v[20:23], v[80:83], v[244:247], v[20:23]\n"
    "v_mfma_f32_16x16x32_bf16 v[24:27], v[84:87], v[240:243], v[24:27]\n"
    "v_mfma_f32_16x16x32_bf16 v[28:31], v[84:87], v[244:247], v[28:31]\n"
    "s_waitcnt lgkmcnt(4)\n"
    "v_mfma_f32_16x16x32_bf16 v[32:35], v[88:91], v[240:243], v[32:35]\n"
    "ds_read_b64_tr_b16 v[72:73], v108 offset:8192\n"
    "ds_read_b64_tr_b16 v[74:75], v108 offset:9216\n"
    "v_mfma_f32_16x16x32_bf16 v[36:39], v[88:91], v[244:247], v[36:39]\n"
    "ds_read_b64_tr_b16 v[76:77], v109 offset:8192\n"
    "ds_read_b64_tr_b16 v[78:79], v109 offset:9216\n"
    "v_mfma_f32_16x16x32_bf16 v[40:43], v[92:95], v[240:243], v[40:43]\n"
    "ds_read_b64_tr_b16 v[80:81], v110 offset:8192\n"
    "ds_read_b64_tr_b16 v[82:83], v110 offset:9216\n"
    "v_mfma_f32_16x16x32_bf16 v[44:47], v[92:95], v[244:247], v[44:47]\n"
    "ds_read_b64_tr_b16 v[84:85], v111 offset:8192\n"
    "s_waitcnt lgkmcnt(7)\n"
    "v_mfma_f32_16x16x32_bf16 v[48:51], v[96:99], v[240:243], v[48:51]\n"
    "ds_read_b64_tr_b16 v[86:87], v111 offset:9216\n"
    "v_mfma_f32_16x16x32_bf16 v[52:55], v[96:99], v[244:247], v[52:55]\n"
    "v_mfma_f32_16x16x32_bf16 v[56:59], v[100:103], v[240:243], v[56:59]\n"
    "v_mfma_f32_16x16x32_bf16 v[60:63], v[100:103], v[244:247], v[60:63]\n"
    "s_waitcnt lgkmcnt(4)\n"
    "v_mfma_f32_16x16x32_bf16 v[0:3], v[72:75], v[248:251], v[0:3]\n"
    "ds_read_b64_tr_b16 v[88:89], v112 offset:8192\n"
    "ds_read_b64_tr_b16 v[90:91], v112 offset:9216\n"
    "v_mfma_f32_16x16x32_bf16 v[4:7], v[72:75], v[252:255], v[4:7]\n"
    "ds_read_b64_tr_b16 v[92:93], v113 offset:8192\n"
    "ds_read_b64_tr_b16 v[94:95], v113 offset:9216\n"
    "v_mfma_f32_16x16x32_bf16 v[64:67], v[72:75], v[104:107], v[64:67]\n"
    "ds_read_b64_tr_b16 v[96:97], v114 offset:8192\n"
    "ds_read_b64_tr_b16 v[98:99], v114 offset:9216\n"
    "v_mfma_f32_16x16x32_bf16 v[8:11], v[76:79], v[248:251], v[8:11]\n"
    "ds_read_b64_tr_b16 v[100:101], v115 offset:8192\n"
    "ds_read_b64_tr_b16 v[102:103], v115 offset:9216\n"
    "v_mfma_f32_16x16x32_bf16 v[12:15], v[76:79], v[252:255], v[12:15]\n"
    "v_mfma_f32_16x16x32_bf16 v[68:71], v[76:79], v[104:107], v[68:71]\n"
    "s_waitcnt lgkmcnt(8)\n"
    "v_mfma_f32_16x16x32_bf16 v[16:19], v[80:83], v[248:251], v[16:19]\n"
    "v_mfma_f32_16x16x32_bf16 v[20:23], v[80:83], v[252:255], v[20:23]\n"
    "v_mfma_f32_16x16x32_bf16 v[24:27], v[84:87], v[248:251], v[24:27]\n"
    "v_mfma_f32_16x16x32_bf16 v[28:31], v[84:87], v[252:255], v[28:31]\n"
    "s_waitcnt lgkmcnt(0)\n"
    "s_barrier\n"
    "v_mfma_f32_16x16x32_bf16 v[32:35], v[88:91], v[248:251], v[32:35]\n"
    "s_mov_b32 exec_lo, 0x10001\n"
    "s_mov_b32 exec_hi, 0x10001\n"
    "ds_write_b128 v125, v[64:67]\n"
    "ds_write_b128 v125, v[68:71] offset:64\n"
    "s_mov_b64 exec, -1\n"
    "v_mfma_f32_16x16x32_bf16 v[36:39], v[88:91], v[252:255], v[36:39]\n"
    "v_pk_mul_f32 v[0:1], v[0:1], v[116:117] op_sel_hi:[1,0]\n"
    "v_pk_mul_f32 v[2:3], v[2:3], v[116:117] op_sel_hi:[1,0]\n"
    "v_mfma_f32_16x16x32_bf16 v[40:43], v[92:95], v[248:251], v[40:43]\n"
    "v_pk_mul_f32 v[4:5], v[4:5], v[118:119] op_sel_hi:[1,0]\n"
    "v_pk_mul_f32 v[6:7], v[6:7], v[118:119] op_sel_hi:[1,0]\n"
    "v_mfma_f32_16x16x32_bf16 v[44:47], v[92:95], v[252:255], v[44:47]\n"
    "v_pk_mul_f32 v[8:9], v[8:9], v[116:117] op_sel_hi:[1,0]\n"
    "v_pk_mul_f32 v[10:11], v[10:11], v[116:117] op_sel_hi:[1,0]\n"
    "v_mfma_f32_16x16x32_bf16 v[48:51], v[96:99], v[248:251], v[48:51]\n"
    "v_pk_mul_f32 v[12:13], v[12:13], v[118:119] op_sel_hi:[1,0]\n"
    "v_pk_mul_f32 v[14:15], v[14:15], v[118:119] op_sel_hi:[1,0]\n"
    "v_mfma_f32_16x16x32_bf16 v[52:55], v[96:99], v[252:255], v[52:55]\n"
    "v_pk_mul_f32 v[16:17], v[16:17], v[116:117] op_sel_hi:[1,0]\n"
    "v_pk_mul_f32 v[18:19], v[18:19], v[116:117] op_sel_hi:[1,0]\n"
    "v_mfma_f32_16x16x32_bf16 v[56:59], v[100:103], v[248:251], v[56:59]\n"
    "v_pk_mul_f32 v[20:21], v[20:21], v[118:119] op_sel_hi:[1,0]\n"
    "v_pk_mul_f32 v[22:23], v[22:23], v[118:119] op_sel_hi:[1,0]\n"
    "v_pk_mul_f32 v[24:25], v[24:25], v[116:117] op_sel_hi:[1,0]\n"
    "v_mfma_f32_16x16x32_bf16 v[60:63], v[100:103], v[252:255], v[60:63]\n"
    "v_pk_mul_f32 v[26:27], v[26:27], v[116:117] op_sel_hi:[1,0]\n"
    "v_pk_mul_f32 v[28:29], v[28:29], v[118:119] op_sel_hi:[1,0]\n"
    "v_pk_mul_f32 v[30:31], v[30:31], v[118:119] op_sel_hi:[1,0]\n"
    "v_add_u32 v160, s64, v122\n"
    "v_add_u32 v161, s65, v122\n"
    "v_add_u32 v162, s66, v122\n"
    "v_add_u32 v163, s67, v122\n"
    "v_pk_mul_f32 v[32:33], v[32:33], v[116:117] op_sel_hi:[1,0]\n"
    "v_pk_mul_f32 v[34:35], v[34:35], v[116:117] op_sel_hi:[1,0]\n"
    "v_pk_mul_f32 v[36:37], v[36:37], v[118:119] op_sel_hi:[1,0]\n"
    "v_pk_mul_f32 v[38:39], v[38:39], v[118:119] op_sel_hi:[1,0]\n"
    "v_pk_mul_f32 v[40:41], v[40:41], v[116:117] op_sel_hi:[1,0]\n"
    "v_pk_mul_f32 v[42:43], v[42:43], v[116:117] op_sel_hi:[1,0]\n"
    "v_pk_mul_f32 v[44:45], v[44:45], v[118:119] op_sel_hi:[1,0]\n"
    "v_pk_mul_f32 v[46:47], v[46:47], v[118:119] op_sel_hi:[1,0]\n"
    "v_pk_mul_f32 v[48:49], v[48:49], v[116:117] op_sel_hi:[1,0]\n"
    "v_pk_mul_f32 v[50:51], v[50:51], v[116:117] op_sel_hi:[1,0]\n"
    "v_pk_mul_f32 v[52:53], v[52:53], v[118:119] op_sel_hi:[1,0]\n"
    "v_pk_mul_f32 v[54:55], v[54:55], v[118:119] op_sel_hi:[1,0]\n"
    "v_pk_mul_f32 v[56:57], v[56:57], v[116:117] op_sel_hi:[1,0]\n"
    "v_pk_mul_f32 v[58:59], v[58:59], v[116:117] op_sel_hi:[1,0]\n"
    "v_pk_mul_f32 v[60:61], v[60:61], v[118:119] op_sel_hi:[1,0]\n"
    "v_pk_mul_f32 v[62:63], v[62:63], v[118:119] op_sel_hi:[1,0]\n"
    "s_waitcnt lgkmcnt(0)\n"
    "s_barrier\n"
    "ds_read_b128 v[176:179], v125 offset:0\n"
    "ds_read_b128 v[180:183], v125 offset:64\n"
    "ds_read_b128 v[184:187], v145 offset:0\n"
    "ds_read_b128 v[188:191], v145 offset:64\n"
    "ds_read_b128 v[92:95], v146 offset:0\n"
    "ds_read_b128 v[96:99], v146 offset:64\n"
    "ds_read_b128 v[104:107], v147 offset:0\n"
    "ds_read_b128 v[108:111], v147 offset:64\n"
    "ds_read_b128 v[64:67], v160 offset:0\n"
    "ds_read_b128 v[68:71], v160 offset:1024\n"
    "ds_read_b128 v[72:75], v160 offset:2048\n"
    "ds_read_b128 v[76:79], v160 offset:3072\n"
    "ds_read_b128 v[80:83], v160 offset:4096\n"
    "ds_read_b128 v[84:87], v160 offset:5120\n"
    "ds_read_b128 v[88:91], v160 offset:6144\n"
    "v_mov_b32 v195, v120\n"
    "v_xor_b32 v196, 64, v120\n"
    "v_add_u32 v197, 81920, v122\n"
    "v_mov_b32 v198, v123\n"
    "v_mov_b32 v199, v126\n"
    "v_mov_b32 v172, v122\n"
    "v_add_u32 v192, 0, v120\n"
    "v_add_u32 v194, 81920, v122\n"
    "s_mov_b32 s79, 0\n"
    "s_mov_b32 s80, 0\n"
    "s_mov_b32 s81, 0\n"
    "buffer_load_dwordx4 v[164:167], v126, s[36:39], 0 offen\n"
    "buffer_load_dwordx4 v[168:171], v126, s[36:39], 0 offen offset:3584\n"
    "s_mov_b32 s82, 0\n"
    "s_getpc_b64 s[84:85]\n"
    ".Lgev0_%=:\n"
    "s_add_u32 s84, s84, .Lev0_%=-.Lgev0_%=\n"
    "s_addc_u32 s85, s85, 0\n"
    "s_waitcnt lgkmcnt(13)\n"
    "v_pk_fma_f32 v[0:1], v[176:177], v[116:117], v[0:1] op_sel:[0,1,0] op_sel_hi:[1,1,1]\n"
    "v_pk_fma_f32 v[2:3], v[178:179], v[116:117], v[2:3] op_sel:[0,1,0] op_sel_hi:[1,1,1]\n"
    "v_pk_fma_f32 v[8:9], v[180:181], v[116:117], v[8:9] op_sel:[0,1,0] op_sel_hi:[1,1,1]\n"
    "v_pk_fma_f32 v[10:11], v[182:183], v[116:117], v[10:11] op_sel:[0,1,0] op_sel_hi:[1,1,1]\n"
    "v_pk_fma_f32 v[4:5], v[176:177], v[118:119], v[4:5] op_sel:[0,1,0] op_sel_hi:[1,1,1]\n"
    "v_pk_fma_f32 v[6:7], v[178:179], v[118:119], v[6:7] op_sel:[0,1,0] op_sel_hi:[1,1,1]\n"
    "v_pk_fma_f32 v[12:13], v[180:181], v[118:119], v[12:13] op_sel:[0,1,0] op_sel_hi:[1,1,1]\n"
    "v_pk_fma_f32 v[14:15], v[182:183], v[118:119], v[14:15] op_sel:[0,1,0] op_sel_hi:[1,1,1]\n"
    "v_cvt_pk_bf16_f32 v128, v0, v1\n"
    "v_cvt_pk_bf16_f32 v129, v2, v3\n"
    "v_cvt_pk_bf16_f32 v130, v8, v9\n"
    "v_cvt_pk_bf16_f32 v131, v10, v11\n"
    "v_pk_max_i16 v128, v128, 0\n"
    "v_pk_max_i16 v129, v129, 0\n"
    "v_pk_max_i16 v130, v130, 0\n"
    "v_pk_max_i16 v131, v131, 0\n"
    "buffer_store_dwordx4 v[128:131], v198, s[52:55], s60 offen offset:0 sc1\n"
    "v_cvt_pk_bf16_f32 v132, v4, v5\n"
    "v_cvt_pk_bf16_f32 v133, v6, v7\n"
    "v_cvt_pk_bf16_f32 v134, v12, v13\n"
    "v_cvt_pk_bf16_f32 v135, v14, v15\n"
    "v_pk_max_i16 v132, v132, 0\n"
    "v_pk_max_i16 v133, v133, 0\n"
    "v_pk_max_i16 v134, v134, 0\n"
    "v_pk_max_i16 v135, v135, 0\n"
    "s_waitcnt lgkmcnt(11)\n"
    "v_pk_fma_f32 v[16:17], v[184:185], v[116:117], v[16:17] op_sel:[0,1,0] op_sel_hi:[1,1,1]\n"
    "v_pk_fma_f32 v[18:19], v[186:187], v[116:117], v[18:19] op_sel:[0,1,0] op_sel_hi:[1,1,1]\n"
    "v_pk_fma_f32 v[24:25], v[188:189], v[116:117], v[24:25] op_sel:[0,1,0] op_sel_hi:[1,1,1]\n"
    "v_pk_fma_f32 v[26:27], v[190:191], v[116:117], v[26:27] op_sel:[0,1,0] op_sel_hi:[1,1,1]\n"
    "v_pk_fma_f32 v[20:21], v[184:185], v[118:119], v[20:21] op_sel:[0,1,0] op_sel_hi:[1,1,1]\n"
    "v_pk_fma_f32 v[22:23], v[186:187], v[118:119], v[22:23] op_sel:[0,1,0] op_sel_hi:[1,1,1]\n"
    "v_pk_fma_f32 v[28:29], v[188:189], v[118:119], v[28:29] op_sel:[0,1,0] op_sel_hi:[1,1,1]\n"
    "v_pk_fma_f32 v[30:31], v[190:191], v[118:119], v[30:31] op_sel:[0,1,0] op_sel_hi:[1,1,1]\n"
    "v_cvt_pk_bf16_f32 v136, v16, v17\n"
    "v_cvt_pk_bf16_f32 v137, v18, v19\n"
    "v_cvt_pk_bf16_f32 v138, v24, v25\n"
    "v_cvt_pk_bf16_f32 v139, v26, v27\n"
    "v_pk_max_i16 v136, v136, 0\n"
    "v_pk_max_i16 v137, v137, 0\n"
    "v_pk_max_i16 v138, v138, 0\n"
    "v_pk_max_i16 v139, v139, 0\n"
    "v_cvt_pk_bf16_f32 v140, v20, v21\n"
    "v_cvt_pk_bf16_f32 v141, v22, v23\n"
    "v_cvt_pk_bf16_f32 v142, v28, v29\n"
    "v_cvt_pk_bf16_f32 v143, v30, v31\n"
    "v_pk_max_i16 v140, v140, 0\n"
    "v_pk_max_i16 v141, v141, 0\n"
    "v_pk_max_i16 v142, v142, 0\n"
    "v_pk_max_i16 v143, v143, 0\n"
    "s_waitcnt lgkmcnt(9)\n"
    "v_pk_fma_f32 v[32:33], v[92:93], v[116:117], v[32:33] op_sel:[0,1,0] op_sel_hi:[1,1,1]\n"
    "v_pk_fma_f32 v[34:35], v[94:95], v[116:117], v[34:35] op_sel:[0,1,0] op_sel_hi:[1,1,1]\n"
    "v_pk_fma_f32 v[40:41], v[96:97], v[116:117], v[40:41] op_sel:[0,1,0] op_sel_hi:[1,1,1]\n"
    "v_pk_fma_f32 v[42:43], v[98:99], v[116:117], v[42:43] op_sel:[0,1,0] op_sel_hi:[1,1,1]\n"
    "v_pk_fma_f32 v[36:37], v[92:93], v[118:119], v[36:37] op_sel:[0,1,0] op_sel_hi:[1,1,1]\n"
    "v_pk_fma_f32 v[38:39], v[94:95], v[118:119], v[38:39] op_sel:[0,1,0] op_sel_hi:[1,1,1]\n"
    "v_pk_fma_f32 v[44:45], v[96:97], v[118:119], v[44:45] op_sel:[0,1,0] op_sel_hi:[1,1,1]\n"
    "v_pk_fma_f32 v[46:47], v[98:99], v[118:119], v[46:47] op_sel:[0,1,0] op_sel_hi:[1,1,1]\n"
    "v_cvt_pk_bf16_f32 v144, v32, v33\n"
    "v_cvt_pk_bf16_f32 v145, v34, v35\n"
    "v_cvt_pk_bf16_f32 v146, v40, v41\n"
    "v_cvt_pk_bf16_f32 v147, v42, v43\n"
    "v_pk_max_i16 v144, v144, 0\n"
    "v_pk_max_i16 v145, v145, 0\n"
    "v_pk_max_i16 v146, v146, 0\n"
    "v_pk_max_i16 v147, v147, 0\n"
    "buffer_store_dwordx4 v[132:135], v198, s[52:55], s60 offen offset:512 sc1\n"
    "v_cvt_pk_bf16_f32 v148, v36, v37\n"
    "v_cvt_pk_bf16_f32 v149, v38, v39\n"
    "v_cvt_pk_bf16_f32 v150, v44, v45\n"
    "v_cvt_pk_bf16_f32 v151, v46, v47\n"
    "v_pk_max_i16 v148, v148, 0\n"
    "v_pk_max_i16 v149, v149, 0\n"
    "v_pk_max_i16 v150, v150, 0\n"
    "v_pk_max_i16 v151, v151, 0\n"
    "s_waitcnt lgkmcnt(7)\n"
    "v_pk_fma_f32 v[48:49], v[104:105], v[116:117], v[48:49] op_sel:[0,1,0] op_sel_hi:[1,1,1]\n"
    "v_pk_fma_f32 v[50:51], v[106:107], v[116:117], v[50:51] op_sel:[0,1,0] op_sel_hi:[1,1,1]\n"
    "v_pk_fma_f32 v[56:57], v[108:109], v[116:117], v[56:57] op_sel:[0,1,0] op_sel_hi:[1,1,1]\n"
    "v_pk_fma_f32 v[58:59], v[110:111], v[116:117], v[58:59] op_sel:[0,1,0] op_sel_hi:[1,1,1]\n"
    "v_pk_fma_f32 v[52:53], v[104:105], v[118:119], v[52:53] op_sel:[0,1,0] op_sel_hi:[1,1,1]\n"
    "v_pk_fma_f32 v[54:55], v[106:107], v[118:119], v[54:55] op_sel:[0,1,0] op_sel_hi:[1,1,1]\n"
    "v_pk_fma_f32 v[60:61], v[108:109], v[118:119], v[60:61] op_sel:[0,1,0] op_sel_hi:[1,1,1]\n"
    "v_pk_fma_f32 v[62:63], v[110:111], v[118:119], v[62:63] op_sel:[0,1,0] op_sel_hi:[1,1,1]\n"
    "v_cvt_pk_bf16_f32 v152, v48, v49\n"
    "v_cvt_pk_bf16_f32 v153, v50, v51\n"
    "v_cvt_pk_bf16_f32 v154, v56, v57\n"
    "v_cvt_pk_bf16_f32 v155, v58, v59\n"
    "v_pk_max_i16 v152, v152, 0\n"
    "v_pk_max_i16 v153, v153, 0\n"
    "v_pk_max_i16 v154, v154, 0\n"
    "v_pk_max_i16 v155, v155, 0\n"
    "v_cvt_pk_bf16_f32 v156, v52, v53\n"
    "v_cvt_pk_bf16_f32 v157, v54, v55\n"
    "v_cvt_pk_bf16_f32 v158, v60, v61\n"
    "v_cvt_pk_bf16_f32 v159, v62, v63\n"
    "v_pk_max_i16 v156, v156, 0\n"
    "v_pk_max_i16 v157, v157, 0\n"
    "v_pk_max_i16 v158, v158, 0\n"
    "v_pk_max_i16 v159, v159, 0\n"
    "s_cmp_eq_u32 s30, 1\n"
    "s_cbranch_scc1 .Lcone_%=\n"
    ".Lcl0_%=:\n"
    "s_waitcnt lgkmcnt(0)\n"
    "v_mfma_f32_16x16x32_bf16 v[200:203], v[64:67], v[128:131], 0\n"
    "ds_read_b128 v[100:103], v161 offset:0\n"
    "v_mfma_f32_16x16x32_bf16 v[204:207], v[64:67], v[132:135], 0\n"
    "ds_read_b128 v[104:107], v161 offset:1024\n"
    "v_mfma_f32_16x16x32_bf16 v[208:211], v[68:71], v[128:131], 0\n"
    "ds_read_b128 v[108:111], v161 offset:2048\n"
    "v_mfma_f32_16x16x32_bf16 v[212:215], v[68:71], v[132:135], 0\n"
    "ds_read_b128 v[112:115], v161 offset:3072\n"
    "v_mfma_f32_16x16x32_bf16 v[216:219], v[72:75], v[128:131], 0\n"
    "ds_read_b128 v[116:119], v161 offset:4096\n"
    "v_mfma_f32_16x16x32_bf16 v[220:223], v[72:75], v[132:135], 0\n"
    "ds_read_b128 v[120:123], v161 offset:5120\n"
    "v_mfma_f32_16x16x32_bf16 v[224:227], v[76:79], v[128:131], 0\n"
    "ds_read_b128 v[124:127], v161 offset:6144\n"
    "v_mfma_f32_16x16x32_bf16 v[228:231], v[76:79], v[132:135], 0\n"
    "buffer_store_dwordx4 v[136:139], v198, s[52:55], s61 offen offset:0 sc1\n"
    "v_mfma_f32_16x16x32_bf16 v[232:235], v[80:83], v[128:131], 0\n"
    "v_mfma_f32_16x16x32_bf16 v[236:239], v[80:83], v[132:135], 0\n"
    "v_mfma_f32_16x16x32_bf16 v[240:243], v[84:87], v[128:131], 0\n"
    "v_mfma_f32_16x16x32_bf16 v[244:247], v[84:87], v[132:135], 0\n"
    "v_mfma_f32_16x16x32_bf16 v[248:251], v[88:91], v[128:131], 0\n"
    "v_mfma_f32_16x16x32_bf16 v[252:255], v[88:91], v[132:135], 0\n"
    "s_cmp_eq_u32 s30, 1\n"
    "s_cbranch_scc1 .Lclast_%=\n"
    "s_waitcnt lgkmcnt(0)\n"
    "v_mfma_f32_16x16x32_bf16 v[200:203], v[100:103], v[136:139], v[200:203]\n"
    "ds_read_b128 v[64:67], v162 offset:0\n"
    "v_mfma_f32_16x16x32_bf16 v[204:207], v[100:103], v[140:143], v[204:207]\n"
    "ds_read_b128 v[68:71], v162 offset:1024\n"
    "v_mfma_f32_16x16x32_bf16 v[208:211], v[104:107], v[136:139], v[208:211]\n"
    "ds_read_b128 v[72:75], v162 offset:2048\n"
    "v_mfma_f32_16x16x32_bf16 v[212:215], v[104:107], v[140:143], v[212:215]\n"
    "ds_read_b128 v[76:79], v162 offset:3072\n"
    "v_mfma_f32_16x16x32_bf16 v[216:219], v[108:111], v[136:139], v[216:219]\n"
    "ds_read_b128 v[80:83], v162 offset:4096\n"
    "v_mfma_f32_16x16x32_bf16 v[220:223], v[108:111], v[140:143], v[220:223]\n"
    "ds_read_b128 v[84:87], v162 offset:5120\n"
    "v_mfma_f32_16x16x32_bf16 v[224:227], v[112:115], v[136:139], v[224:227]\n"
    "ds_read_b128 v[88:91], v162 offset:6144\n"
    "v_mfma_f32_16x16x32_bf16 v[228:231], v[112:115], v[140:143], v[228:231]\n"
    "buffer_store_dwordx4 v[140:143], v198, s[52:55], s61 offen offset:512 sc1\n"
    "v_mfma_f32_16x16x32_bf16 v[232:235], v[116:119], v[136:139], v[232:235]\n"
    "v_mfma_f32_16x16x32_bf16 v[236:239], v[116:119], v[140:143], v[236:239]\n"
    "v_mfma_f32_16x16x32_bf16 v[240:243], v[120:123], v[136:139], v[240:243]\n"
    "v_mfma_f32_16x16x32_bf16 v[244:247], v[120:123], v[140:143], v[244:247]\n"
    "v_mfma_f32_16x16x32_bf16 v[248:251], v[124:127], v[136:139], v[248:251]\n"
    "v_mfma_f32_16x16x32_bf16 v[252:255], v[124:127], v[140:143], v[252:255]\n"
    "s_waitcnt lgkmcnt(0)\n"
    "v_mfma_f32_16x16x32_bf16 v[200:203], v[64:67], v[144:147], v[200:203]\n"
    "ds_read_b128 v[100:103], v163 offset:0\n"
    "v_mfma_f32_16x16x32_bf16 v[204:207], v[64:67], v[148:151], v[204:207]\n"
    "ds_read_b128 v[104:107], v163 offset:1024\n"
    "v_mfma_f32_16x16x32_bf16 v[208:211], v[68:71], v[144:147], v[208:211]\n"
    "ds_read_b128 v[108:111], v163 offset:2048\n"
    "v_mfma_f32_16x16x32_bf16 v[212:215], v[68:71], v[148:151], v[212:215]\n"
    "ds_read_b128 v[112:115], v163 offset:3072\n"
    "v_mfma_f32_16x16x32_bf16 v[216:219], v[72:75], v[144:147], v[216:219]\n"
    "ds_read_b128 v[116:119], v163 offset:4096\n"
    "v_mfma_f32_16x16x32_bf16 v[220:223], v[72:75], v[148:151], v[220:223]\n"
    "ds_read_b128 v[120:123], v163 offset:5120\n"
    "v_mfma_f32_16x16x32_bf16 v[224:227], v[76:79], v[144:147], v[224:227]\n"
    "ds_read_b128 v[124:127], v163 offset:6144\n"
    "v_mfma_f32_16x16x32_bf16 v[228:231], v[76:79], v[148:151], v[228:231]\n"
    "buffer_store_dwordx4 v[144:147], v198, s[52:55], s62 offen offset:0 sc1\n"
    "v_mfma_f32_16x16x32_bf16 v[232:235], v[80:83], v[144:147], v[232:235]\n"
    "v_mfma_f32_16x16x32_bf16 v[236:239], v[80:83], v[148:151], v[236:239]\n"
    "v_mfma_f32_16x16x32_bf16 v[240:243], v[84:87], v[144:147], v[240:243]\n"
    "v_mfma_f32_16x16x32_bf16 v[244:247], v[84:87], v[148:151], v[244:247]\n"
    "v_mfma_f32_16x16x32_bf16 v[248:251], v[88:91], v[144:147], v[248:251]\n"
    "v_mfma_f32_16x16x32_bf16 v[252:255], v[88:91], v[148:151], v[252:255]\n"
    ".Lclast_%=:\n"
    "s_waitcnt lgkmcnt(0)\n"
    "s_barrier\n"
    "v_mfma_f32_16x16x32_bf16 v[200:203], v[100:103], v[152:155], v[200:203]\n"
    "ds_read_b128 v[56:59], v192 offset:0\n"
    "v_mfma_f32_16x16x32_bf16 v[204:207], v[100:103], v[156:159], v[204:207]\n"
    "ds_read_b128 v[64:67], v194 offset:0\n"
    "v_mfma_f32_16x16x32_bf16 v[208:211], v[104:107], v[152:155], v[208:211]\n"
    "ds_read_b128 v[60:63], v192 offset:512\n"
    "v_mfma_f32_16x16x32_bf16 v[212:215], v[104:107], v[156:159], v[212:215]\n"
    "ds_read_b128 v[68:71], v194 offset:1024\n"
    "v_mfma_f32_16x16x32_bf16 v[216:219], v[108:111], v[152:155], v[216:219]\n"
    "ds_read_b128 v[72:75], v194 offset:2048\n"
    "v_mfma_f32_16x16x32_bf16 v[220:223], v[108:111], v[156:159], v[220:223]\n"
    "ds_read_b128 v[76:79], v194 offset:3072\n"
    "v_mfma_f32_16x16x32_bf16 v[224:227], v[112:115], v[152:155], v[224:227]\n"
    "ds_read_b128 v[80:83], v194 offset:4096\n"
    "v_mfma_f32_16x16x32_bf16 v[228:231], v[112:115], v[156:159], v[228:231]\n"
    "ds_read_b128 v[84:87], v194 offset:5120\n"
    "v_mfma_f32_16x16x32_bf16 v[232:235], v[116:119], v[152:155], v[232:235]\n"
    "ds_read_b128 v[88:91], v194 offset:6144\n"
    "v_mfma_f32_16x16x32_bf16 v[236:239], v[116:119], v[156:159], v[236:239]\n"
    "v_add_u32 v193, s79, v196\n"
    "s_add_u32 s79, s79, 16384\n"
    "v_mfma_f32_16x16x32_bf16 v[240:243], v[120:123], v[152:155], v[240:243]\n"
    "s_add_u32 s80, s80, 14336\n"
    "s_cmp_eq_u32 s79, 81920\n"
    "v_mfma_f32_16x16x32_bf16 v[244:247], v[120:123], v[156:159], v[244:247]\n"
    "s_cselect_b32 s79, 0, s79\n"
    "s_cselect_b32 s80, 0, s80\n"
    "v_mfma_f32_16x16x32_bf16 v[248:251], v[124:127], v[152:155], v[248:251]\n"
    "buffer_store_dwordx4 v[148:151], v198, s[52:55], s62 offen offset:512 sc1\n"
    "v_mfma_f32_16x16x32_bf16 v[252:255], v[124:127], v[156:159], v[252:255]\n"
    ".p2align 3\n"
    ".Lcloop_%=:\n"
    "s_waitcnt lgkmcnt(3)\n"
    "v_mfma_f32_16x16x32_bf16 v[200:203], v[64:67], v[56:59], v[200:203]\n"
    "ds_read_b128 v[92:95], v193 offset:0\n"
    "ds_read_b128 v[100:103], v194 offset:7168\n"
    "v_mfma_f32_16x16x32_bf16 v[204:207], v[64:67], v[60:63], v[204:207]\n"
    "ds_read_b128 v[96:99], v193 offset:512\n"
    "ds_read_b128 v[104:107], v194 offset:8192\n"
    "v_mfma_f32_16x16x32_bf16 v[208:211], v[68:71], v[56:59], v[208:211]\n"
    "ds_read_b128 v[108:111], v194 offset:9216\n"
    "v_mfma_f32_16x16x32_bf16 v[212:215], v[68:71], v[60:63], v[212:215]\n"
    "ds_read_b128 v[112:115], v194 offset:10240\n"
    "v_mfma_f32_16x16x32_bf16 v[216:219], v[72:75], v[56:59], v[216:219]\n"
    "ds_read_b128 v[116:119], v194 offset:11264\n"
    "v_mfma_f32_16x16x32_bf16 v[220:223], v[72:75], v[60:63], v[220:223]\n"
    "ds_read_b128 v[120:123], v194 offset:12288\n"
    "v_mfma_f32_16x16x32_bf16 v[224:227], v[76:79], v[56:59], v[224:227]\n"
    "ds_read_b128 v[124:127], v194 offset:13312\n"
    "v_mfma_f32_16x16x32_bf16 v[228:231], v[76:79], v[60:63], v[228:231]\n"
    "v_add_u32 v192, s79, v195\n"
    "s_waitcnt lgkmcnt(9)\n"
    "v_mfma_f32_16x16x32_bf16 v[232:235], v[80:83], v[56:59], v[232:235]\n"
    "v_add_u32 v194, s80, v197\n"
    "v_mfma_f32_16x16x32_bf16 v[236:239], v[80:83], v[60:63], v[236:239]\n"
    "s_cmp_eq_u32 s81, s82\n"
    "v_mfma_f32_16x16x32_bf16 v[240:243], v[84:87], v[56:59], v[240:243]\n"
    "v_mfma_f32_16x16x32_bf16 v[244:247], v[84:87], v[60:63], v[244:247]\n"
    "v_mfma_f32_16x16x32_bf16 v[248:251], v[88:91], v[56:59], v[248:251]\n"
    "v_mfma_f32_16x16x32_bf16 v[252:255], v[88:91], v[60:63], v[252:255]\n"
    "s_cbranch_scc1 .Lcev_%=\n"
    ".Lcmid_%=:\n"
    "s_waitcnt lgkmcnt(0)\n"
    "s_barrier\n"
    "v_mfma_f32_16x16x32_bf16 v[200:203], v[100:103], v[92:95], v[200:203]\n"
    "ds_read_b128 v[56:59], v192 offset:0\n"
    "v_mfma_f32_16x16x32_bf16 v[204:207], v[100:103], v[96:99], v[204:207]\n"
    "ds_read_b128 v[64:67], v194 offset:0\n"
    "v_mfma_f32_16x16x32_bf16 v[208:211], v[104:107], v[92:95], v[208:211]\n"
    "ds_read_b128 v[60:63], v192 offset:512\n"
    "v_mfma_f32_16x16x32_bf16 v[212:215], v[104:107], v[96:99], v[212:215]\n"
    "ds_read_b128 v[68:71], v194 offset:1024\n"
    "v_mfma_f32_16x16x32_bf16 v[216:219], v[108:111], v[92:95], v[216:219]\n"
    "ds_read_b128 v[72:75], v194 offset:2048\n"
    "v_mfma_f32_16x16x32_bf16 v[220:223], v[108:111], v[96:99], v[220:223]\n"
    "ds_read_b128 v[76:79], v194 offset:3072\n"
    "v_mfma_f32_16x16x32_bf16 v[224:227], v[112:115], v[92:95], v[224:227]\n"
    "ds_read_b128 v[80:83], v194 offset:4096\n"
    "v_mfma_f32_16x16x32_bf16 v[228:231], v[112:115], v[96:99], v[228:231]\n"
    "ds_read_b128 v[84:87], v194 offset:5120\n"
    "v_mfma_f32_16x16x32_bf16 v[232:235], v[116:119], v[92:95], v[232:235]\n"
    "ds_read_b128 v[88:91], v194 offset:6144\n"
    "v_mfma_f32_16x16x32_bf16 v[236:239], v[116:119], v[96:99], v[236:239]\n"
    "v_add_u32 v193, s79, v196\n"
    "s_add_u32 s79, s79, 16384\n"
    "v_mfma_f32_16x16x32_bf16 v[240:243], v[120:123], v[92:95], v[240:243]\n"
    "s_add_u32 s80, s80, 14336\n"
    "s_cmp_eq_u32 s79, 81920\n"
    "v_mfma_f32_16x16x32_bf16 v[244:247], v[120:123], v[96:99], v[244:247]\n"
    "s_cselect_b32 s79, 0, s79\n"
    "v_mfma_f32_16x16x32_bf16 v[248:251], v[124:127], v[92:95], v[248:251]\n"
    "s_cselect_b32 s80, 0, s80\n"
    "v_mfma_f32_16x16x32_bf16 v[252:255], v[124:127], v[96:99], v[252:255]\n"
    "s_add_u32 s81, s81, 1\n"
    "s_branch .Lcloop_%=\n"
    ".Lcev_%=:\n"
    "s_setpc_b64 s[84:85]\n"
    ".Lev0_%=:\n"
    "buffer_store_dwordx4 v[152:155], v198, s[52:55], s63 offen offset:0 sc1\n"
    "s_mov_b32 s82, 1\n"
    "s_add_u32 s84, s84, .Lev1_%=-.Lev0_%=\n"
    "s_addc_u32 s85, s85, 0\n"
    "s_branch .Lcmid_%=\n"
    ".Lev1_%=:\n"
    "buffer_store_dwordx4 v[156:159], v198, s[52:55], s63 offen offset:512 sc1\n"
    "s_mov_b32 s82, 2\n"
    "s_add_u32 s84, s84, .Lev2_%=-.Lev1_%=\n"
    "s_addc_u32 s85, s85, 0\n"
    "s_branch .Lcmid_%=\n"
    ".Lev2_%=:\n"
    "s_cmp_eq_u32 s30, 1\n"
    "s_cbranch_scc0 .Lsk0_%=\n"
    "buffer_store_dwordx4 v[140:143], v198, s[52:55], s61 offen offset:512 sc1\n"
    ".Lsk0_%=:\n"
    "s_mov_b32 s82, 3\n"
    "s_add_u32 s84, s84, .Lev3_%=-.Lev2_%=\n"
    "s_addc_u32 s85, s85, 0\n"
    "s_branch .Lcmid_%=\n"
    ".Lev3_%=:\n"
    "s_cmp_eq_u32 s30, 1\n"
    "s_cbranch_scc0 .Lsk1_%=\n"
    "buffer_store_dwordx4 v[144:147], v198, s[52:55], s62 offen offset:0 sc1\n"
    ".Lsk1_%=:\n"
    "s_mov_b32 s82, 5\n"
    "s_add_u32 s84, s84, .Lev4_%=-.Lev3_%=\n"
    "s_addc_u32 s85, s85, 0\n"
    "s_branch .Lcmid_%=\n"
    ".Lev4_%=:\n"
    "s_waitcnt vmcnt(0)\n"
    "s_mov_b32 s82, 15\n"
    "s_add_u32 s84, s84, .LevF_%=-.Lev4_%=\n"
    "s_addc_u32 s85, s85, 0\n"
    "s_branch .Lcmid_%=\n"
    ".LevF_%=:\n"
    "v_mov_b32 v195, v198\n"
    "v_add_u32 v196, 2048, v198\n"
    "v_add_u32 v192, s79, v198\n"
    "v_bfe_u32 v190, v172, 4, 2\n"
    "v_lshlrev_b32 v190, 1, v190\n"
    "v_add_u32 v190, 154632, v190\n"
    "ds_read_u16 v190, v190\n"
    "s_mov_b32 s82, s78\n"
    "s_add_u32 s84, s84, .Lctail_%=-.LevF_%=\n"
    "s_addc_u32 s85, s85, 0\n"
    "s_branch .Lcmid_%=\n"
    ".Lcone_%=:\n"
    "s_nop 1\n"
    "s_cmp_lt_u32 s22, 2\n"
    "s_cbranch_scc1 .Lcone2_%=\n"
    "v_mov_b32 v128, v144\n"
    "v_mov_b32 v129, v145\n"
    "v_mov_b32 v130, v146\n"
    "v_mov_b32 v131, v147\n"
    "v_mov_b32 v132, v148\n"
    "v_mov_b32 v133, v149\n"
    "v_mov_b32 v134, v150\n"
    "v_mov_b32 v135, v151\n"
    "s_nop 1\n"
    "s_branch .Lcl0_%=\n"
    ".Lcone2_%=:\n"
    "v_swap_b32 v152, v136\n"
    "v_swap_b32 v153, v137\n"
    "v_swap_b32 v154, v138\n"
    "v_swap_b32 v155, v139\n"
    "v_swap_b32 v156, v140\n"
    "v_swap_b32 v157, v141\n"
    "v_swap_b32 v158, v142\n"
    "v_swap_b32 v159, v143\n"
    "s_mov_b32 s31, s63\n"
    "s_mov_b32 s63, s61\n"
    "s_mov_b32 s61, s31\n"
    "s_branch .Lcl0_%=\n"
    ".Lcidle_%=:\n"
    "v_lshl_add_u32 v6, v2, 2, v3\n"
    "v_mul_u32_u24 v6, 896, v6\n"
    "v_lshl_add_u32 v6, v4, 4, v6\n"
    "s_lshr_b32 s31, s22, 1\n"
    "s_and_b32 s32, s22, 1\n"
    "s_mul_i32 s33, s31, 28672\n"
    "s_mul_i32 s34, s32, 448\n"
    "s_add_u32 s33, s33, s34\n"
    "s_add_u32 s33, s33, 81920\n"
    "v_add_u32 v6, s33, v6\n"
    "s_lshr_b32 s33, s24, 1\n"
    "s_lshl1_add_u32 s33, s27, s33\n"
    "s_lshl1_add_u32 s34, s33, s31\n"
    "s_mul_i32 s32, s32, 112\n"
    "s_add_u32 s34, s34, s32\n"
    "s_mul_i32 s34, s34, 7168\n"
    "s_add_u32 s56, s34, 5242880\n"
    "s_add_u32 s57, s56, 4096\n"
    "s_add_u32 s58, s56, 401408\n"
    "s_add_u32 s59, s58, 4096\n"
    "v_mov_b32 v195, v120\n"
    "v_xor_b32 v196, 64, v120\n"
    "v_add_u32 v197, 81920, v122\n"
    "v_mov_b32 v198, v123\n"
    "v_mov_b32 v199, v126\n"
    "v_mov_b32 v172, v122\n"
    "v_add_u32 v192, 0, v120\n"
    "v_add_u32 v194, 81920, v122\n"
    "s_mov_b32 s79, 0\n"
    "s_mov_b32 s80, 0\n"
    "s_mov_b32 s81, 0\n"
    "buffer_load_dwordx4 v[164:167], v126, s[36:39], 0 offen\n"
    "buffer_load_dwordx4 v[168:171], v126, s[36:39], 0 offen offset:3584\n"
    "s_mov_b32 s82, 15\n"
    "s_getpc_b64 s[84:85]\n"
    ".LgevF_%=:\n"
    "s_sub_u32 s84, s84, .LgevF_%=-.LevF_%=\n"
    "s_subb_u32 s85, s85, 0\n"
    "v_mov_b32 v200, 0\n"
    "v_mov_b32 v201, 0\n"
    "v_mov_b32 v202, 0\n"
    "v_mov_b32 v203, 0\n"
    "v_mov_b32 v204, 0\n"
    "v_mov_b32 v205, 0\n"
    "v_mov_b32 v206, 0\n"
    "v_mov_b32 v207, 0\n"
    "v_mov_b32 v208, 0\n"
    "v_mov_b32 v209, 0\n"
    "v_mov_b32 v210, 0\n"
    "v_mov_b32 v211, 0\n"
    "v_mov_b32 v212, 0\n"
    "v_mov_b32 v213, 0\n"
    "v_mov_b32 v214, 0\n"
    "v_mov_b32 v215, 0\n"
    "v_mov_b32 v216, 0\n"
    "v_mov_b32 v217, 0\n"
    "v_mov_b32 v218, 0\n"
    "v_mov_b32 v219, 0\n"
    "v_mov_b32 v220, 0\n"
    "v_mov_b32 v221, 0\n"
    "v_mov_b32 v222, 0\n"
    "v_mov_b32 v223, 0\n"
    "v_mov_b32 v224, 0\n"
    "v_mov_b32 v225, 0\n"
    "v_mov_b32 v226, 0\n"
    "v_mov_b32 v227, 0\n"
    "v_mov_b32 v228, 0\n"
    "v_mov_b32 v229, 0\n"
    "v_mov_b32 v230, 0\n"
    "v_mov_b32 v231, 0\n"
    "v_mov_b32 v232, 0\n"
    "v_mov_b32 v233, 0\n"
    "v_mov_b32 v234, 0\n"
    "v_mov_b32 v235, 0\n"
    "v_mov_b32 v236, 0\n"
    "v_mov_b32 v237, 0\n"
    "v_mov_b32 v238, 0\n"
    "v_mov_b32 v239, 0\n"
    "v_mov_b32 v240, 0\n"
    "v_mov_b32 v241, 0\n"
    "v_mov_b32 v242, 0\n"
    "v_mov_b32 v243, 0\n"
    "v_mov_b32 v244, 0\n"
    "v_mov_b32 v245, 0\n"
    "v_mov_b32 v246, 0\n"
    "v_mov_b32 v247, 0\n"
    "v_mov_b32 v248, 0\n"
    "v_mov_b32 v249, 0\n"
    "v_mov_b32 v250, 0\n"
    "v_mov_b32 v251, 0\n"
    "v_mov_b32 v252, 0\n"
    "v_mov_b32 v253, 0\n"
    "v_mov_b32 v254, 0\n"
    "v_mov_b32 v255, 0\n"
    "s_barrier\n"
    "s_cmp_ge_u32 s33, 28\n"
    "s_cbranch_scc1 .Lcid2_%=\n"
    "ds_read_b64_tr_b16 v[8:9], v6 offset:0\n"
    "ds_read_b64_tr_b16 v[10:11], v6 offset:14336\n"
    "ds_read_b64_tr_b16 v[12:13], v6 offset:8\n"
    "ds_read_b64_tr_b16 v[14:15], v6 offset:14344\n"
    "ds_read_b64_tr_b16 v[16:17], v6 offset:64\n"
    "ds_read_b64_tr_b16 v[18:19], v6 offset:14400\n"
    "ds_read_b64_tr_b16 v[20:21], v6 offset:72\n"
    "ds_read_b64_tr_b16 v[22:23], v6 offset:14408\n"
    "ds_read_b64_tr_b16 v[24:25], v6 offset:128\n"
    "ds_read_b64_tr_b16 v[26:27], v6 offset:14464\n"
    "ds_read_b64_tr_b16 v[28:29], v6 offset:136\n"
    "ds_read_b64_tr_b16 v[30:31], v6 offset:14472\n"
    "ds_read_b64_tr_b16 v[32:33], v6 offset:192\n"
    "ds_read_b64_tr_b16 v[34:35], v6 offset:14528\n"
    "s_waitcnt lgkmcnt(12)\n"
    "buffer_store_dwordx4 v[8:11], v122, s[52:55], s56 offen offset:0 sc1\n"
    "s_waitcnt lgkmcnt(10)\n"
    "buffer_store_dwordx4 v[12:15], v122, s[52:55], s56 offen offset:1024 sc1\n"
    "s_waitcnt lgkmcnt(8)\n"
    "buffer_store_dwordx4 v[16:19], v122, s[52:55], s56 offen offset:2048 sc1\n"
    "s_waitcnt lgkmcnt(6)\n"
    "buffer_store_dwordx4 v[20:23], v122, s[52:55], s56 offen offset:3072 sc1\n"
    "s_waitcnt lgkmcnt(4)\n"
    "buffer_store_dwordx4 v[24:27], v122, s[52:55], s57 offen offset:0 sc1\n"
    "s_waitcnt lgkmcnt(2)\n"
    "buffer_store_dwordx4 v[28:31], v122, s[52:55], s57 offen offset:1024 sc1\n"
    "s_waitcnt lgkmcnt(0)\n"
    "buffer_store_dwordx4 v[32:35], v122, s[52:55], s57 offen offset:2048 sc1\n"
    "ds_read_b64_tr_b16 v[36:37], v6 offset:200\n"
    "ds_read_b64_tr_b16 v[38:39], v6 offset:14536\n"
    "ds_read_b64_tr_b16 v[40:41], v6 offset:256\n"
    "ds_read_b64_tr_b16 v[42:43], v6 offset:14592\n"
    "ds_read_b64_tr_b16 v[44:45], v6 offset:264\n"
    "ds_read_b64_tr_b16 v[46:47], v6 offset:14600\n"
    "ds_read_b64_tr_b16 v[48:49], v6 offset:320\n"
    "ds_read_b64_tr_b16 v[50:51], v6 offset:14656\n"
    "ds_read_b64_tr_b16 v[52:53], v6 offset:328\n"
    "ds_read_b64_tr_b16 v[54:55], v6 offset:14664\n"
    "ds_read_b64_tr_b16 v[56:57], v6 offset:384\n"
    "ds_read_b64_tr_b16 v[58:59], v6 offset:14720\n"
    "ds_read_b64_tr_b16 v[60:61], v6 offset:392\n"
    "ds_read_b64_tr_b16 v[62:63], v6 offset:14728\n"
    "s_waitcnt lgkmcnt(12)\n"
    "buffer_store_dwordx4 v[36:39], v122, s[52:55], s59 offen offset:2048 sc1\n"
    "s_waitcnt lgkmcnt(10)\n"
    "buffer_store_dwordx4 v[40:43], v122, s[52:55], s58 offen offset:0 sc1\n"
    "s_waitcnt lgkmcnt(8)\n"
    "buffer_store_dwordx4 v[44:47], v122, s[52:55], s58 offen offset:1024 sc1\n"
    "s_waitcnt lgkmcnt(6)\n"
    "buffer_store_dwordx4 v[48:51], v122, s[52:55], s58 offen offset:2048 sc1\n"
    "s_waitcnt lgkmcnt(4)\n"
    "buffer_store_dwordx4 v[52:55], v122, s[52:55], s58 offen offset:3072 sc1\n"
    "s_waitcnt lgkmcnt(2)\n"
    "buffer_store_dwordx4 v[56:59], v122, s[52:55], s59 offen offset:0 sc1\n"
    "s_waitcnt lgkmcnt(0)\n"
    "buffer_store_dwordx4 v[60:63], v122, s[52:55], s59 offen offset:1024 sc1\n"
    ".Lcid2_%=:\n"
    "s_waitcnt vmcnt(0)\n"
    "s_barrier\n"
    "s_barrier\n"
    "s_barrier\n"
    "s_barrier\n"
    "ds_read_b128 v[56:59], v192 offset:0\n"
    "ds_read_b128 v[64:67], v194 offset:0\n"
    "ds_read_b128 v[60:63], v192 offset:512\n"
    "ds_read_b128 v[68:71], v194 offset:1024\n"
    "ds_read_b128 v[72:75], v194 offset:2048\n"
    "ds_read_b128 v[76:79], v194 offset:3072\n"
    "ds_read_b128 v[80:83], v194 offset:4096\n"
    "ds_read_b128 v[84:87], v194 offset:5120\n"
    "ds_read_b128 v[88:91], v194 offset:6144\n"
    "v_add_u32 v193, s79, v196\n"
    "s_add_u32 s79, s79, 16384\n"
    "s_add_u32 s80, s80, 14336\n"
    "s_cmp_eq_u32 s79, 81920\n"
    "s_cselect_b32 s79, 0, s79\n"
    "s_cselect_b32 s80, 0, s80\n"
    "s_branch .Lcloop_%=\n"
    ".Lctail_%=:\n"
    "s_add_u32 s79, s79, 16384\n"
    "s_add_u32 s80, s80, 14336\n"
    "s_cmp_eq_u32 s79, 81920\n"
    "s_cselect_b32 s79, 0, s79\n"
    "s_cselect_b32 s80, 0, s80\n"
    "v_add_u32 v180, s79, v198\n"
    "v_add_u32 v182, s80, v197\n"
    "s_add_u32 s79, s79, 16384\n"
    "s_add_u32 s80, s80, 14336\n"
    "s_cmp_eq_u32 s79, 81920\n"
    "s_cselect_b32 s79, 0, s79\n"
    "s_cselect_b32 s80, 0, s80\n"
    "v_add_u32 v181, s79, v198\n"
    "v_add_u32 v183, s80, v197\n"
    "v_lshl_or_b32 v187, v190, 16, v190\n"
    "v_lshlrev_b32 v190, 16, v190\n"
    "v_lshrrev_b32 v176, 2, v172\n"
    "s_mov_b32 s75, 0x07060302\n"
    "s_mov_b32 s76, 7\n"
    "s_mov_b32 s83, 16384\n"
    "s_cmp_eq_u32 s24, 0\n"
    "s_cbranch_scc1 .Lct0_%=\n"
    "s_cmp_eq_u32 s24, 1\n"
    "s_cbranch_scc1 .Lct1_%=\n"
    "s_cmp_eq_u32 s24, 2\n"
    "s_cbranch_scc1 .Lct2_%=\n"
    "s_waitcnt lgkmcnt(0)\n"
    "s_barrier\n"
    "v_mfma_f32_16x16x32_bf16 v[200:203], v[100:103], v[92:95], v[200:203]\n"
    "ds_read_b128 v[0:3], v192 offset:0\n"
    "ds_read_b128 v[56:59], v194 offset:0\n"
    "v_mfma_f32_16x16x32_bf16 v[204:207], v[100:103], v[96:99], v[204:207]\n"
    "ds_read_b128 v[4:7], v192 offset:512\n"
    "ds_read_b128 v[8:11], v192 offset:2048\n"
    "v_mfma_f32_16x16x32_bf16 v[208:211], v[104:107], v[92:95], v[208:211]\n"
    "ds_read_b128 v[60:63], v194 offset:7168\n"
    "ds_read_b128 v[12:15], v192 offset:2560\n"
    "v_mfma_f32_16x16x32_bf16 v[212:215], v[104:107], v[96:99], v[212:215]\n"
    "ds_read_b128 v[16:19], v180 offset:0\n"
    "ds_read_b128 v[64:67], v182 offset:0\n"
    "v_mfma_f32_16x16x32_bf16 v[216:219], v[108:111], v[92:95], v[216:219]\n"
    "ds_read_b128 v[20:23], v180 offset:512\n"
    "v_mfma_f32_16x16x32_bf16 v[220:223], v[108:111], v[96:99], v[220:223]\n"
    "ds_read_b128 v[24:27], v180 offset:2048\n"
    "v_mfma_f32_16x16x32_bf16 v[224:227], v[112:115], v[92:95], v[224:227]\n"
    "ds_read_b128 v[68:71], v182 offset:7168\n"
    "v_mfma_f32_16x16x32_bf16 v[228:231], v[112:115], v[96:99], v[228:231]\n"
    "ds_read_b128 v[28:31], v180 offset:2560\n"
    "v_mfma_f32_16x16x32_bf16 v[232:235], v[116:119], v[92:95], v[232:235]\n"
    "ds_read_b128 v[32:35], v181 offset:0\n"
    "v_mfma_f32_16x16x32_bf16 v[236:239], v[116:119], v[96:99], v[236:239]\n"
    "ds_read_b128 v[72:75], v183 offset:0\n"
    "v_mfma_f32_16x16x32_bf16 v[240:243], v[120:123], v[92:95], v[240:243]\n"
    "ds_read_b128 v[36:39], v181 offset:512\n"
    "v_mfma_f32_16x16x32_bf16 v[244:247], v[120:123], v[96:99], v[244:247]\n"
    "s_waitcnt lgkmcnt(9)\n"
    "ds_read_b128 v[40:43], v181 offset:2048\n"
    "v_mfma_f32_16x16x32_bf16 v[248:251], v[124:127], v[92:95], v[248:251]\n"
    "ds_read_b128 v[76:79], v183 offset:7168\n"
    "v_mfma_f32_16x16x32_bf16 v[252:255], v[124:127], v[96:99], v[252:255]\n"
    "ds_read_b128 v[44:47], v181 offset:2560\n"
    "v_mfma_f32_16x16x32_bf16 v[200:203], v[56:59], v[0:3], v[200:203]\n"
    "ds_read_b128 v[92:95], v194 offset:1024\n"
    "v_mfma_f32_16x16x32_bf16 v[204:207], v[56:59], v[4:7], v[204:207]\n"
    "ds_read_b128 v[96:99], v194 offset:8192\n"
    "v_mfma_f32_16x16x32_bf16 v[200:203], v[60:63], v[8:11], v[200:203]\n"
    "ds_read_b128 v[100:103], v182 offset:1024\n"
    "s_waitcnt lgkmcnt(9)\n"
    "ds_read_b128 v[104:107], v182 offset:8192\n"
    "v_mfma_f32_16x16x32_bf16 v[204:207], v[60:63], v[12:15], v[204:207]\n"
    "ds_read_b128 v[108:111], v183 offset:1024\n"
    "ds_read_b128 v[112:115], v183 offset:8192\n"
    "v_mfma_f32_16x16x32_bf16 v[200:203], v[64:67], v[16:19], v[200:203]\n"
    "v_mfma_f32_16x16x32_bf16 v[204:207], v[64:67], v[20:23], v[204:207]\n"
    "v_mfma_f32_16x16x32_bf16 v[200:203], v[68:71], v[24:27], v[200:203]\n"
    "v_mfma_f32_16x16x32_bf16 v[204:207], v[68:71], v[28:31], v[204:207]\n"
    "s_waitcnt lgkmcnt(6)\n"
    "v_mfma_f32_16x16x32_bf16 v[200:203], v[72:75], v[32:35], v[200:203]\n"
    "v_mfma_f32_16x16x32_bf16 v[204:207], v[72:75], v[36:39], v[204:207]\n"
    "v_mfma_f32_16x16x32_bf16 v[200:203], v[76:79], v[40:43], v[200:203]\n"
    "v_mfma_f32_16x16x32_bf16 v[204:207], v[76:79], v[44:47], v[204:207]\n"
    "s_waitcnt lgkmcnt(3)\n"
    "v_mfma_f32_16x16x32_bf16 v[208:211], v[92:95], v[0:3], v[208:211]\n"
    "ds_read_b128 v[56:59], v194 offset:2048\n"
    "v_mfma_f32_16x16x32_bf16 v[212:215], v[92:95], v[4:7], v[212:215]\n"
    "ds_read_b128 v[60:63], v194 offset:9216\n"
    "v_mfma_f32_16x16x32_bf16 v[208:211], v[96:99], v[8:11], v[208:211]\n"
    "v_cvt_pk_bf16_f32 v51, v206, v207\n"
    "v_cvt_pk_bf16_f32 v48, v200, v201\n"
    "v_mfma_f32_16x16x32_bf16 v[212:215], v[96:99], v[12:15], v[212:215]\n"
    "v_cvt_pk_bf16_f32 v49, v202, v203\n"
    "v_cvt_pk_bf16_f32 v50, v204, v205\n"
    "v_mfma_f32_16x16x32_bf16 v[208:211], v[100:103], v[16:19], v[208:211]\n"
    "v_xor_b32 v51, v51, v190\n"
    "v_mfma_f32_16x16x32_bf16 v[212:215], v[100:103], v[20:23], v[212:215]\n"
    "buffer_store_dwordx4 v[48:51], v172, s[52:55], s72 offen offset:0 sc1\n"
    "s_waitcnt lgkmcnt(2)\n"
    "v_mfma_f32_16x16x32_bf16 v[208:211], v[104:107], v[24:27], v[208:211]\n"
    "ds_read_b128 v[64:67], v182 offset:2048\n"
    "v_mfma_f32_16x16x32_bf16 v[212:215], v[104:107], v[28:31], v[212:215]\n"
    "ds_read_b128 v[68:71], v182 offset:9216\n"
    "v_mfma_f32_16x16x32_bf16 v[208:211], v[108:111], v[32:35], v[208:211]\n"
    "ds_read_b128 v[72:75], v183 offset:2048\n"
    "v_mfma_f32_16x16x32_bf16 v[212:215], v[108:111], v[36:39], v[212:215]\n"
    "ds_read_b128 v[76:79], v183 offset:9216\n"
    "v_mfma_f32_16x16x32_bf16 v[208:211], v[112:115], v[40:43], v[208:211]\n"
    "v_mfma_f32_16x16x32_bf16 v[212:215], v[112:115], v[44:47], v[212:215]\n"
    "s_waitcnt lgkmcnt(3)\n"
    "v_mfma_f32_16x16x32_bf16 v[216:219], v[56:59], v[0:3], v[216:219]\n"
    "ds_read_b128 v[92:95], v194 offset:3072\n"
    "v_mfma_f32_16x16x32_bf16 v[220:223], v[56:59], v[4:7], v[220:223]\n"
    "ds_read_b128 v[96:99], v194 offset:10240\n"
    "v_mfma_f32_16x16x32_bf16 v[216:219], v[60:63], v[8:11], v[216:219]\n"
    "v_cvt_pk_bf16_f32 v55, v214, v215\n"
    "v_cvt_pk_bf16_f32 v52, v208, v209\n"
    "v_mfma_f32_16x16x32_bf16 v[220:223], v[60:63], v[12:15], v[220:223]\n"
    "v_cvt_pk_bf16_f32 v53, v210, v211\n"
    "v_cvt_pk_bf16_f32 v54, v212, v213\n"
    "v_mfma_f32_16x16x32_bf16 v[216:219], v[64:67], v[16:19], v[216:219]\n"
    "v_xor_b32 v55, v55, v190\n"
    "v_mfma_f32_16x16x32_bf16 v[220:223], v[64:67], v[20:23], v[220:223]\n"
    "buffer_store_dwordx4 v[52:55], v172, s[52:55], s72 offen offset:1024 sc1\n"
    "s_waitcnt lgkmcnt(2)\n"
    "v_mfma_f32_16x16x32_bf16 v[216:219], v[68:71], v[24:27], v[216:219]\n"
    "v_cvt_pk_bf16_f32 v188, v207, v215\n"
    "v_mfma_f32_16x16x32_bf16 v[220:223], v[68:71], v[28:31], v[220:223]\n"
    "buffer_store_dword v188, v176, s[52:55], s72 offen offset:2048 sc1\n"
    "v_mfma_f32_16x16x32_bf16 v[216:219], v[72:75], v[32:35], v[216:219]\n"
    "ds_read_b128 v[100:103], v182 offset:3072\n"
    "v_mfma_f32_16x16x32_bf16 v[220:223], v[72:75], v[36:39], v[220:223]\n"
    "ds_read_b128 v[104:107], v182 offset:10240\n"
    "v_mfma_f32_16x16x32_bf16 v[216:219], v[76:79], v[40:43], v[216:219]\n"
    "ds_read_b128 v[108:111], v183 offset:3072\n"
    "v_mfma_f32_16x16x32_bf16 v[220:223], v[76:79], v[44:47], v[220:223]\n"
    "ds_read_b128 v[112:115], v183 offset:10240\n"
    "s_waitcnt lgkmcnt(3)\n"
    "v_mfma_f32_16x16x32_bf16 v[224:227], v[92:95], v[0:3], v[224:227]\n"
    "ds_read_b128 v[56:59], v194 offset:4096\n"
    "v_mfma_f32_16x16x32_bf16 v[228:231], v[92:95], v[4:7], v[228:231]\n"
    "ds_read_b128 v[60:63], v194 offset:11264\n"
    "v_mfma_f32_16x16x32_bf16 v[224:227], v[96:99], v[8:11], v[224:227]\n"
    "v_cvt_pk_bf16_f32 v51, v222, v223\n"
    "v_cvt_pk_bf16_f32 v48, v216, v217\n"
    "v_mfma_f32_16x16x32_bf16 v[228:231], v[96:99], v[12:15], v[228:231]\n"
    "v_cvt_pk_bf16_f32 v49, v218, v219\n"
    "v_cvt_pk_bf16_f32 v50, v220, v221\n"
    "v_mfma_f32_16x16x32_bf16 v[224:227], v[100:103], v[16:19], v[224:227]\n"
    "v_xor_b32 v51, v51, v190\n"
    "v_mfma_f32_16x16x32_bf16 v[228:231], v[100:103], v[20:23], v[228:231]\n"
    "buffer_store_dwordx4 v[48:51], v172, s[52:55], s73 offen offset:0 sc1\n"
    "s_waitcnt lgkmcnt(2)\n"
    "v_mfma_f32_16x16x32_bf16 v[224:227], v[104:107], v[24:27], v[224:227]\n"
    "ds_read_b128 v[64:67], v182 offset:4096\n"
    "v_mfma_f32_16x16x32_bf16 v[228:231], v[104:107], v[28:31], v[228:231]\n"
    "ds_read_b128 v[68:71], v182 offset:11264\n"
    "v_mfma_f32_16x16x32_bf16 v[224:227], v[108:111], v[32:35], v[224:227]\n"
    "ds_read_b128 v[72:75], v183 offset:4096\n"
    "v_mfma_f32_16x16x32_bf16 v[228:231], v[108:111], v[36:39], v[228:231]\n"
    "ds_read_b128 v[76:79], v183 offset:11264\n"
    "v_mfma_f32_16x16x32_bf16 v[224:227], v[112:115], v[40:43], v[224:227]\n"
    "v_mfma_f32_16x16x32_bf16 v[228:231], v[112:115], v[44:47], v[228:231]\n"
    "s_waitcnt lgkmcnt(3)\n"
    "v_mfma_f32_16x16x32_bf16 v[232:235], v[56:59], v[0:3], v[232:235]\n"
    "ds_read_b128 v[92:95], v194 offset:5120\n"
    "v_mfma_f32_16x16x32_bf16 v[236:239], v[56:59], v[4:7], v[236:239]\n"
    "ds_read_b128 v[96:99], v194 offset:12288\n"
    "v_mfma_f32_16x16x32_bf16 v[232:235], v[60:63], v[8:11], v[232:235]\n"
    "v_cvt_pk_bf16_f32 v55, v230, v231\n"
    "v_cvt_pk_bf16_f32 v52, v224, v225\n"
    "v_mfma_f32_16x16x32_bf16 v[236:239], v[60:63], v[12:15], v[236:239]\n"
    "v_cvt_pk_bf16_f32 v53, v226, v227\n"
    "v_cvt_pk_bf16_f32 v54, v228, v229\n"
    "v_mfma_f32_16x16x32_bf16 v[232:235], v[64:67], v[16:19], v[232:235]\n"
    "v_xor_b32 v55, v55, v190\n"
    "v_mfma_f32_16x16x32_bf16 v[236:239], v[64:67], v[20:23], v[236:239]\n"
    "buffer_store_dwordx4 v[52:55], v172, s[52:55], s73 offen offset:1024 sc1\n"
    "s_waitcnt lgkmcnt(2)\n"
    "v_mfma_f32_16x16x32_bf16 v[232:235], v[68:71], v[24:27], v[232:235]\n"
    "v_cvt_pk_bf16_f32 v189, v223, v231\n"
    "v_mfma_f32_16x16x32_bf16 v[236:239], v[68:71], v[28:31], v[236:239]\n"
    "buffer_store_dword v189, v176, s[52:55], s73 offen offset:2048 sc1\n"
    "v_mfma_f32_16x16x32_bf16 v[232:235], v[72:75], v[32:35], v[232:235]\n"
    "ds_read_b128 v[100:103], v182 offset:5120\n"
    "v_mfma_f32_16x16x32_bf16 v[236:239], v[72:75], v[36:39], v[236:239]\n"
    "ds_read_b128 v[104:107], v182 offset:12288\n"
    "v_mfma_f32_16x16x32_bf16 v[232:235], v[76:79], v[40:43], v[232:235]\n"
    "ds_read_b128 v[108:111], v183 offset:5120\n"
    "v_mfma_f32_16x16x32_bf16 v[236:239], v[76:79], v[44:47], v[236:239]\n"
    "ds_read_b128 v[112:115], v183 offset:12288\n"
    "s_waitcnt lgkmcnt(3)\n"
    "v_mfma_f32_16x16x32_bf16 v[240:243], v[92:95], v[0:3], v[240:243]\n"
    "ds_read_b128 v[56:59], v194 offset:6144\n"
    "v_mfma_f32_16x16x32_bf16 v[244:247], v[92:95], v[4:7], v[244:247]\n"
    "ds_read_b128 v[60:63], v194 offset:13312\n"
    "v_mfma_f32_16x16x32_bf16 v[240:243], v[96:99], v[8:11], v[240:243]\n"
    "v_cvt_pk_bf16_f32 v51, v238, v239\n"
    "v_cvt_pk_bf16_f32 v48, v232, v233\n"
    "v_mfma_f32_16x16x32_bf16 v[244:247], v[96:99], v[12:15], v[244:247]\n"
    "v_cvt_pk_bf16_f32 v49, v234, v235\n"
    "v_cvt_pk_bf16_f32 v50, v236, v237\n"
    "v_mfma_f32_16x16x32_bf16 v[240:243], v[100:103], v[16:19], v[240:243]\n"
    "v_xor_b32 v51, v51, v190\n"
    "v_mfma_f32_16x16x32_bf16 v[244:247], v[100:103], v[20:23], v[244:247]\n"
    "buffer_store_dwordx4 v[48:51], v172, s[52:55], s74 offen offset:0 sc1\n"
    "s_waitcnt lgkmcnt(2)\n"
    "v_mfma_f32_16x16x32_bf16 v[240:243], v[104:107], v[24:27], v[240:243]\n"
    "ds_read_b128 v[64:67], v182 offset:6144\n"
    "v_mfma_f32_16x16x32_bf16 v[244:247], v[104:107], v[28:31], v[244:247]\n"
    "ds_read_b128 v[68:71], v182 offset:13312\n"
    "v_mfma_f32_16x16x32_bf16 v[240:243], v[108:111], v[32:35], v[240:243]\n"
    "ds_read_b128 v[72:75], v183 offset:6144\n"
    "v_mfma_f32_16x16x32_bf16 v[244:247], v[108:111], v[36:39], v[244:247]\n"
    "ds_read_b128 v[76:79], v183 offset:13312\n"
    "v_mfma_f32_16x16x32_bf16 v[240:243], v[112:115], v[40:43], v[240:243]\n"
    "v_mfma_f32_16x16x32_bf16 v[244:247], v[112:115], v[44:47], v[244:247]\n"
    "s_waitcnt lgkmcnt(3)\n"
    "v_mfma_f32_16x16x32_bf16 v[248:251], v[56:59], v[0:3], v[248:251]\n"
    "v_mfma_f32_16x16x32_bf16 v[252:255], v[56:59], v[4:7], v[252:255]\n"
    "v_mfma_f32_16x16x32_bf16 v[248:251], v[60:63], v[8:11], v[248:251]\n"
    "v_cvt_pk_bf16_f32 v55, v246, v247\n"
    "v_cvt_pk_bf16_f32 v52, v240, v241\n"
    "v_mfma_f32_16x16x32_bf16 v[252:255], v[60:63], v[12:15], v[252:255]\n"
    "v_cvt_pk_bf16_f32 v53, v242, v243\n"
    "v_cvt_pk_bf16_f32 v54, v244, v245\n"
    "v_mfma_f32_16x16x32_bf16 v[248:251], v[64:67], v[16:19], v[248:251]\n"
    "v_xor_b32 v55, v55, v190\n"
    "buffer_store_dwordx4 v[52:55], v172, s[52:55], s74 offen offset:1024 sc1\n"
    "v_mfma_f32_16x16x32_bf16 v[252:255], v[64:67], v[20:23], v[252:255]\n"
    "v_cvt_pk_bf16_f32 v188, v239, v247\n"
    "buffer_store_dword v188, v176, s[52:55], s74 offen offset:2048 sc1\n"
    "s_waitcnt lgkmcnt(0)\n"
    "v_mfma_f32_16x16x32_bf16 v[248:251], v[68:71], v[24:27], v[248:251]\n"
    "v_lshlrev_b32 v92, 16, v164\n"
    "v_and_b32 v93, 0xffff0000, v164\n"
    "v_mfma_f32_16x16x32_bf16 v[252:255], v[68:71], v[28:31], v[252:255]\n"
    "v_lshlrev_b32 v94, 16, v165\n"
    "v_and_b32 v95, 0xffff0000, v165\n"
    "v_mfma_f32_16x16x32_bf16 v[248:251], v[72:75], v[32:35], v[248:251]\n"
    "v_lshlrev_b32 v96, 16, v168\n"
    "v_and_b32 v97, 0xffff0000, v168\n"
    "v_mfma_f32_16x16x32_bf16 v[252:255], v[72:75], v[36:39], v[252:255]\n"
    "v_lshlrev_b32 v98, 16, v169\n"
    "v_and_b32 v99, 0xffff0000, v169\n"
    "v_mfma_f32_16x16x32_bf16 v[248:251], v[76:79], v[40:43], v[248:251]\n"
    "v_mfma_f32_16x16x32_bf16 v[252:255], v[76:79], v[44:47], v[252:255]\n"
    "s_nop 3\n"
    "v_pk_add_f32 v[0:1], v[248:249], v[92:93]\n"
    "v_pk_add_f32 v[2:3], v[250:251], v[94:95]\n"
    "s_nop 1\n"
    "v_pk_add_f32 v[4:5], v[252:253], v[96:97]\n"
    "v_pk_add_f32 v[6:7], v[254:255], v[98:99]\n"
    "s_sleep 8\n"
    "buffer_load_dwordx4 v[128:131], v172, s[52:55], s68 offen offset:0 sc1\n"
    "buffer_load_dword v136, v176, s[52:55], s68 offen offset:2048 sc1\n"
    "s_sleep 2\n"
    "buffer_load_dwordx4 v[138:141], v172, s[52:55], s69 offen offset:0 sc1\n"
    "buffer_load_dword v146, v176, s[52:55], s69 offen offset:2048 sc1\n"
    "s_sleep 2\n"
    "buffer_load_dwordx4 v[148:151], v172, s[52:55], s70 offen offset:0 sc1\n"
    "buffer_load_dword v156, v176, s[52:55], s70 offen offset:2048 sc1\n"
    "s_waitcnt vmcnt(4)\n"
    "v_perm_b32 v64, v131, v131, s75\n"
    "v_xor_b32 v64, v64, v136\n"
    "v_cmp_ne_u32 vcc, v64, v187\n"
    "s_cbranch_vccnz .Lcfa00_%=\n"
    "v_lshlrev_b32 v16, 16, v128\n"
    "v_and_b32 v17, 0xffff0000, v128\n"
    "v_lshlrev_b32 v18, 16, v129\n"
    "v_and_b32 v19, 0xffff0000, v129\n"
    "v_lshlrev_b32 v20, 16, v130\n"
    "v_and_b32 v21, 0xffff0000, v130\n"
    "v_lshlrev_b32 v22, 16, v131\n"
    "v_lshlrev_b32 v23, 16, v136\n"
    "s_bitset0_b32 s76, 0\n"
    ".Lcfa00_%=:\n"
    "s_waitcnt vmcnt(2)\n"
    "v_perm_b32 v64, v141, v141, s75\n"
    "v_xor_b32 v64, v64, v146\n"
    "v_cmp_ne_u32 vcc, v64, v187\n"
    "s_cbranch_vccnz .Lcfa10_%=\n"
    "v_lshlrev_b32 v32, 16, v138\n"
    "v_and_b32 v33, 0xffff0000, v138\n"
    "v_lshlrev_b32 v34, 16, v139\n"
    "v_and_b32 v35, 0xffff0000, v139\n"
    "v_lshlrev_b32 v36, 16, v140\n"
    "v_and_b32 v37, 0xffff0000, v140\n"
    "v_lshlrev_b32 v38, 16, v141\n"
    "v_lshlrev_b32 v39, 16, v146\n"
    "s_bitset0_b32 s76, 1\n"
    ".Lcfa10_%=:\n"
    "s_waitcnt vmcnt(0)\n"
    "v_perm_b32 v64, v151, v151, s75\n"
    "v_xor_b32 v64, v64, v156\n"
    "v_cmp_ne_u32 vcc, v64, v187\n"
    "s_cbranch_vccnz .Lcfa20_%=\n"
    "v_lshlrev_b32 v48, 16, v148\n"
    "v_and_b32 v49, 0xffff0000, v148\n"
    "v_lshlrev_b32 v50, 16, v149\n"
    "v_and_b32 v51, 0xffff0000, v149\n"
    "v_lshlrev_b32 v52, 16, v150\n"
    "v_and_b32 v53, 0xffff0000, v150\n"
    "v_lshlrev_b32 v54, 16, v151\n"
    "v_lshlrev_b32 v55, 16, v156\n"
    "s_bitset0_b32 s76, 2\n"
    ".Lcfa20_%=:\n"
    "s_cmp_eq_u32 s76, 0\n"
    "s_cbranch_scc1 .Lcd0_%=\n"
    "s_bitcmp1_b32 s76, 0\n"
    "s_cbranch_scc0 .Lcsi00_%=\n"
    "buffer_load_dwordx4 v[128:131], v172, s[52:55], s68 offen offset:0 sc1\n"
    "buffer_load_dword v136, v176, s[52:55], s68 offen offset:2048 sc1\n"
    ".Lcsi00_%=:\n"
    "s_bitcmp1_b32 s76, 1\n"
    "s_cbranch_scc0 .Lcsi01_%=\n"
    "buffer_load_dwordx4 v[138:141], v172, s[52:55], s69 offen offset:0 sc1\n"
    "buffer_load_dword v146, v176, s[52:55], s69 offen offset:2048 sc1\n"
    ".Lcsi01_%=:\n"
    "s_bitcmp1_b32 s76, 2\n"
    "s_cbranch_scc0 .Lcsi02_%=\n"
    "buffer_load_dwordx4 v[148:151], v172, s[52:55], s70 offen offset:0 sc1\n"
    "buffer_load_dword v156, v176, s[52:55], s70 offen offset:2048 sc1\n"
    ".Lcsi02_%=:\n"
    ".Lcl0x_%=:\n"
    "s_waitcnt vmcnt(0)\n"
    "s_bitcmp1_b32 s76, 0\n"
    "s_cbranch_scc0 .Lcsl000_%=\n"
    "v_perm_b32 v64, v131, v131, s75\n"
    "v_xor_b32 v64, v64, v136\n"
    "v_cmp_ne_u32 vcc, v64, v187\n"
    "s_cbranch_vccnz .Lcfb000_%=\n"
    "v_lshlrev_b32 v16, 16, v128\n"
    "v_and_b32 v17, 0xffff0000, v128\n"
    "v_lshlrev_b32 v18, 16, v129\n"
    "v_and_b32 v19, 0xffff0000, v129\n"
    "v_lshlrev_b32 v20, 16, v130\n"
    "v_and_b32 v21, 0xffff0000, v130\n"
    "v_lshlrev_b32 v22, 16, v131\n"
    "v_lshlrev_b32 v23, 16, v136\n"
    "s_bitset0_b32 s76, 0\n"
    "s_branch .Lcnb000_%=\n"
    ".Lcfb000_%=:\n"
    "buffer_load_dwordx4 v[128:131], v172, s[52:55], s68 offen offset:0 sc1\n"
    "buffer_load_dword v136, v176, s[52:55], s68 offen offset:2048 sc1\n"
    ".Lcnb000_%=:\n"
    ".Lcsl000_%=:\n"
    "s_bitcmp1_b32 s76, 1\n"
    "s_cbranch_scc0 .Lcsl001_%=\n"
    "v_perm_b32 v64, v141, v141, s75\n"
    "v_xor_b32 v64, v64, v146\n"
    "v_cmp_ne_u32 vcc, v64, v187\n"
    "s_cbranch_vccnz .Lcfb010_%=\n"
    "v_lshlrev_b32 v32, 16, v138\n"
    "v_and_b32 v33, 0xffff0000, v138\n"
    "v_lshlrev_b32 v34, 16, v139\n"
    "v_and_b32 v35, 0xffff0000, v139\n"
    "v_lshlrev_b32 v36, 16, v140\n"
    "v_and_b32 v37, 0xffff0000, v140\n"
    "v_lshlrev_b32 v38, 16, v141\n"
    "v_lshlrev_b32 v39, 16, v146\n"
    "s_bitset0_b32 s76, 1\n"
    "s_branch .Lcnb010_%=\n"
    ".Lcfb010_%=:\n"
    "buffer_load_dwordx4 v[138:141], v172, s[52:55], s69 offen offset:0 sc1\n"
    "buffer_load_dword v146, v176, s[52:55], s69 offen offset:2048 sc1\n"
    ".Lcnb010_%=:\n"
    ".Lcsl001_%=:\n"
    "s_bitcmp1_b32 s76, 2\n"
    "s_cbranch_scc0 .Lcsl002_%=\n"
    "v_perm_b32 v64, v151, v151, s75\n"
    "v_xor_b32 v64, v64, v156\n"
    "v_cmp_ne_u32 vcc, v64, v187\n"
    "s_cbranch_vccnz .Lcfb020_%=\n"
    "v_lshlrev_b32 v48, 16, v148\n"
    "v_and_b32 v49, 0xffff0000, v148\n"
    "v_lshlrev_b32 v50, 16, v149\n"
    "v_and_b32 v51, 0xffff0000, v149\n"
    "v_lshlrev_b32 v52, 16, v150\n"
    "v_and_b32 v53, 0xffff0000, v150\n"
    "v_lshlrev_b32 v54, 16, v151\n"
    "v_lshlrev_b32 v55, 16, v156\n"
    "s_bitset0_b32 s76, 2\n"
    "s_branch .Lcnb020_%=\n"
    ".Lcfb020_%=:\n"
    "buffer_load_dwordx4 v[148:151], v172, s[52:55], s70 offen offset:0 sc1\n"
    "buffer_load_dword v156, v176, s[52:55], s70 offen offset:2048 sc1\n"
    ".Lcnb020_%=:\n"
    ".Lcsl002_%=:\n"
    "s_cmp_eq_u32 s76, 0\n"
    "s_cbranch_scc1 .Lcd0_%=\n"
    "s_sub_u32 s83, s83, 1\n"
    "s_cbranch_scc0 .Lcl0x_%=\n"
    ".Lcd0_%=:\n"
    "v_pk_add_f32 v[0:1], v[0:1], v[16:17]\n"
    "v_pk_add_f32 v[2:3], v[2:3], v[18:19]\n"
    "v_pk_add_f32 v[4:5], v[4:5], v[20:21]\n"
    "v_pk_add_f32 v[6:7], v[6:7], v[22:23]\n"
    "v_pk_add_f32 v[0:1], v[0:1], v[32:33]\n"
    "v_pk_add_f32 v[2:3], v[2:3], v[34:35]\n"
    "v_pk_add_f32 v[4:5], v[4:5], v[36:37]\n"
    "v_pk_add_f32 v[6:7], v[6:7], v[38:39]\n"
    "v_pk_add_f32 v[0:1], v[0:1], v[48:49]\n"
    "v_pk_add_f32 v[2:3], v[2:3], v[50:51]\n"
    "v_pk_add_f32 v[4:5], v[4:5], v[52:53]\n"
    "v_pk_add_f32 v[6:7], v[6:7], v[54:55]\n"
    "v_cvt_pk_bf16_f32 v72, v0, v1\n"
    "v_cvt_pk_bf16_f32 v73, v2, v3\n"
    "buffer_store_dwordx2 v[72:73], v199, s[40:43], 0 offen offset:0\n"
    "v_cvt_pk_bf16_f32 v76, v4, v5\n"
    "v_cvt_pk_bf16_f32 v77, v6, v7\n"
    "buffer_store_dwordx2 v[76:77], v199, s[40:43], 0 offen offset:3584\n"
    "s_endpgm\n"
    ".Lct0_%=:\n"
    "s_waitcnt lgkmcnt(0)\n"
    "s_barrier\n"
    "v_mfma_f32_16x16x32_bf16 v[200:203], v[100:103], v[92:95], v[200:203]\n"
    "ds_read_b128 v[0:3], v192 offset:0\n"
    "ds_read_b128 v[56:59], v194 offset:2048\n"
    "v_mfma_f32_16x16x32_bf16 v[204:207], v[100:103], v[96:99], v[204:207]\n"
    "ds_read_b128 v[4:7], v192 offset:512\n"
    "ds_read_b128 v[8:11], v192 offset:2048\n"
    "v_mfma_f32_16x16x32_bf16 v[208:211], v[104:107], v[92:95], v[208:211]\n"
    "ds_read_b128 v[60:63], v194 offset:9216\n"
    "ds_read_b128 v[12:15], v192 offset:2560\n"
    "v_mfma_f32_16x16x32_bf16 v[212:215], v[104:107], v[96:99], v[212:215]\n"
    "ds_read_b128 v[16:19], v180 offset:0\n"
    "ds_read_b128 v[64:67], v182 offset:2048\n"
    "v_mfma_f32_16x16x32_bf16 v[216:219], v[108:111], v[92:95], v[216:219]\n"
    "ds_read_b128 v[20:23], v180 offset:512\n"
    "v_mfma_f32_16x16x32_bf16 v[220:223], v[108:111], v[96:99], v[220:223]\n"
    "ds_read_b128 v[24:27], v180 offset:2048\n"
    "v_mfma_f32_16x16x32_bf16 v[224:227], v[112:115], v[92:95], v[224:227]\n"
    "ds_read_b128 v[68:71], v182 offset:9216\n"
    "v_mfma_f32_16x16x32_bf16 v[228:231], v[112:115], v[96:99], v[228:231]\n"
    "ds_read_b128 v[28:31], v180 offset:2560\n"
    "v_mfma_f32_16x16x32_bf16 v[232:235], v[116:119], v[92:95], v[232:235]\n"
    "ds_read_b128 v[32:35], v181 offset:0\n"
    "v_mfma_f32_16x16x32_bf16 v[236:239], v[116:119], v[96:99], v[236:239]\n"
    "ds_read_b128 v[72:75], v183 offset:2048\n"
    "v_mfma_f32_16x16x32_bf16 v[240:243], v[120:123], v[92:95], v[240:243]\n"
    "ds_read_b128 v[36:39], v181 offset:512\n"
    "v_mfma_f32_16x16x32_bf16 v[244:247], v[120:123], v[96:99], v[244:247]\n"
    "s_waitcnt lgkmcnt(9)\n"
    "ds_read_b128 v[40:43], v181 offset:2048\n"
    "v_mfma_f32_16x16x32_bf16 v[248:251], v[124:127], v[92:95], v[248:251]\n"
    "ds_read_b128 v[76:79], v183 offset:9216\n"
    "v_mfma_f32_16x16x32_bf16 v[252:255], v[124:127], v[96:99], v[252:255]\n"
    "ds_read_b128 v[44:47], v181 offset:2560\n"
    "v_mfma_f32_16x16x32_bf16 v[216:219], v[56:59], v[0:3], v[216:219]\n"
    "ds_read_b128 v[92:95], v194 offset:3072\n"
    "v_mfma_f32_16x16x32_bf16 v[220:223], v[56:59], v[4:7], v[220:223]\n"
    "ds_read_b128 v[96:99], v194 offset:10240\n"
    "v_mfma_f32_16x16x32_bf16 v[216:219], v[60:63], v[8:11], v[216:219]\n"
    "ds_read_b128 v[100:103], v182 offset:3072\n"
    "s_waitcnt lgkmcnt(9)\n"
    "ds_read_b128 v[104:107], v182 offset:10240\n"
    "v_mfma_f32_16x16x32_bf16 v[220:223], v[60:63], v[12:15], v[220:223]\n"
    "ds_read_b128 v[108:111], v183 offset:3072\n"
    "ds_read_b128 v[112:115], v183 offset:10240\n"
    "v_mfma_f32_16x16x32_bf16 v[216:219], v[64:67], v[16:19], v[216:219]\n"
    "v_mfma_f32_16x16x32_bf16 v[220:223], v[64:67], v[20:23], v[220:223]\n"
    "v_mfma_f32_16x16x32_bf16 v[216:219], v[68:71], v[24:27], v[216:219]\n"
    "v_mfma_f32_16x16x32_bf16 v[220:223], v[68:71], v[28:31], v[220:223]\n"
    "s_waitcnt lgkmcnt(6)\n"
    "v_mfma_f32_16x16x32_bf16 v[216:219], v[72:75], v[32:35], v[216:219]\n"
    "v_mfma_f32_16x16x32_bf16 v[220:223], v[72:75], v[36:39], v[220:223]\n"
    "v_mfma_f32_16x16x32_bf16 v[216:219], v[76:79], v[40:43], v[216:219]\n"
    "v_mfma_f32_16x16x32_bf16 v[220:223], v[76:79], v[44:47], v[220:223]\n"
    "s_waitcnt lgkmcnt(3)\n"
    "v_mfma_f32_16x16x32_bf16 v[224:227], v[92:95], v[0:3], v[224:227]\n"
    "ds_read_b128 v[56:59], v194 offset:4096\n"
    "v_mfma_f32_16x16x32_bf16 v[228:231], v[92:95], v[4:7], v[228:231]\n"
    "ds_read_b128 v[60:63], v194 offset:11264\n"
    "v_mfma_f32_16x16x32_bf16 v[224:227], v[96:99], v[8:11], v[224:227]\n"
    "v_cvt_pk_bf16_f32 v51, v222, v223\n"
    "v_cvt_pk_bf16_f32 v48, v216, v217\n"
    "v_mfma_f32_16x16x32_bf16 v[228:231], v[96:99], v[12:15], v[228:231]\n"
    "v_cvt_pk_bf16_f32 v49, v218, v219\n"
    "v_cvt_pk_bf16_f32 v50, v220, v221\n"
    "v_mfma_f32_16x16x32_bf16 v[224:227], v[100:103], v[16:19], v[224:227]\n"
    "v_xor_b32 v51, v51, v190\n"
    "v_mfma_f32_16x16x32_bf16 v[228:231], v[100:103], v[20:23], v[228:231]\n"
    "buffer_store_dwordx4 v[48:51], v172, s[52:55], s72 offen offset:0 sc1\n"
    "s_waitcnt lgkmcnt(2)\n"
    "v_mfma_f32_16x16x32_bf16 v[224:227], v[104:107], v[24:27], v[224:227]\n"
    "ds_read_b128 v[64:67], v182 offset:4096\n"
    "v_mfma_f32_16x16x32_bf16 v[228:231], v[104:107], v[28:31], v[228:231]\n"
    "ds_read_b128 v[68:71], v182 offset:11264\n"
    "v_mfma_f32_16x16x32_bf16 v[224:227], v[108:111], v[32:35], v[224:227]\n"
    "ds_read_b128 v[72:75], v183 offset:4096\n"
    "v_mfma_f32_16x16x32_bf16 v[228:231], v[108:111], v[36:39], v[228:231]\n"
    "ds_read_b128 v[76:79], v183 offset:11264\n"
    "v_mfma_f32_16x16x32_bf16 v[224:227], v[112:115], v[40:43], v[224:227]\n"
    "v_mfma_f32_16x16x32_bf16 v[228:231], v[112:115], v[44:47], v[228:231]\n"
    "s_waitcnt lgkmcnt(3)\n"
    "v_mfma_f32_16x16x32_bf16 v[232:235], v[56:59], v[0:3], v[232:235]\n"
    "ds_read_b128 v[92:95], v194 offset:5120\n"
    "v_mfma_f32_16x16x32_bf16 v[236:239], v[56:59], v[4:7], v[236:239]\n"
    "ds_read_b128 v[96:99], v194 offset:12288\n"
    "v_mfma_f32_16x16x32_bf16 v[232:235], v[60:63], v[8:11], v[232:235]\n"
    "v_cvt_pk_bf16_f32 v55, v230, v231\n"
    "v_cvt_pk_bf16_f32 v52, v224, v225\n"
    "v_mfma_f32_16x16x32_bf16 v[236:239], v[60:63], v[12:15], v[236:239]\n"
    "v_cvt_pk_bf16_f32 v53, v226, v227\n"
    "v_cvt_pk_bf16_f32 v54, v228, v229\n"
    "v_mfma_f32_16x16x32_bf16 v[232:235], v[64:67], v[16:19], v[232:235]\n"
    "v_xor_b32 v55, v55, v190\n"
    "v_mfma_f32_16x16x32_bf16 v[236:239], v[64:67], v[20:23], v[236:239]\n"
    "buffer_store_dwordx4 v[52:55], v172, s[52:55], s72 offen offset:1024 sc1\n"
    "s_waitcnt lgkmcnt(2)\n"
    "v_mfma_f32_16x16x32_bf16 v[232:235], v[68:71], v[24:27], v[232:235]\n"
    "v_cvt_pk_bf16_f32 v188, v223, v231\n"
    "v_mfma_f32_16x16x32_bf16 v[236:239], v[68:71], v[28:31], v[236:239]\n"
    "buffer_store_dword v188, v176, s[52:55], s72 offen offset:2048 sc1\n"
    "v_mfma_f32_16x16x32_bf16 v[232:235], v[72:75], v[32:35], v[232:235]\n"
    "ds_read_b128 v[100:103], v182 offset:5120\n"
    "v_mfma_f32_16x16x32_bf16 v[236:239], v[72:75], v[36:39], v[236:239]\n"
    "ds_read_b128 v[104:107], v182 offset:12288\n"
    "v_mfma_f32_16x16x32_bf16 v[232:235], v[76:79], v[40:43], v[232:235]\n"
    "ds_read_b128 v[108:111], v183 offset:5120\n"
    "v_mfma_f32_16x16x32_bf16 v[236:239], v[76:79], v[44:47], v[236:239]\n"
    "ds_read_b128 v[112:115], v183 offset:12288\n"
    "s_waitcnt lgkmcnt(3)\n"
    "v_mfma_f32_16x16x32_bf16 v[240:243], v[92:95], v[0:3], v[240:243]\n"
    "ds_read_b128 v[56:59], v194 offset:6144\n"
    "v_mfma_f32_16x16x32_bf16 v[244:247], v[92:95], v[4:7], v[244:247]\n"
    "ds_read_b128 v[60:63], v194 offset:13312\n"
    "v_mfma_f32_16x16x32_bf16 v[240:243], v[96:99], v[8:11], v[240:243]\n"
    "v_cvt_pk_bf16_f32 v51, v238, v239\n"
    "v_cvt_pk_bf16_f32 v48, v232, v233\n"
    "v_mfma_f32_16x16x32_bf16 v[244:247], v[96:99], v[12:15], v[244:247]\n"
    "v_cvt_pk_bf16_f32 v49, v234, v235\n"
    "v_cvt_pk_bf16_f32 v50, v236, v237\n"
    "v_mfma_f32_16x16x32_bf16 v[240:243], v[100:103], v[16:19], v[240:243]\n"
    "v_xor_b32 v51, v51, v190\n"
    "v_mfma_f32_16x16x32_bf16 v[244:247], v[100:103], v[20:23], v[244:247]\n"
    "buffer_store_dwordx4 v[48:51], v172, s[52:55], s73 offen offset:0 sc1\n"
    "s_waitcnt lgkmcnt(2)\n"
    "v_mfma_f32_16x16x32_bf16 v[240:243], v[104:107], v[24:27], v[240:243]\n"
    "ds_read_b128 v[64:67], v182 offset:6144\n"
    "v_mfma_f32_16x16x32_bf16 v[244:247], v[104:107], v[28:31], v[244:247]\n"
    "ds_read_b128 v[68:71], v182 offset:13312\n"
    "v_mfma_f32_16x16x32_bf16 v[240:243], v[108:111], v[32:35], v[240:243]\n"
    "ds_read_b128 v[72:75], v183 offset:6144\n"
    "v_mfma_f32_16x16x32_bf16 v[244:247], v[108:111], v[36:39], v[244:247]\n"
    "ds_read_b128 v[76:79], v183 offset:13312\n"
    "v_mfma_f32_16x16x32_bf16 v[240:243], v[112:115], v[40:43], v[240:243]\n"
    "v_mfma_f32_16x16x32_bf16 v[244:247], v[112:115], v[44:47], v[244:247]\n"
    "s_waitcnt lgkmcnt(3)\n"
    "v_mfma_f32_16x16x32_bf16 v[248:251], v[56:59], v[0:3], v[248:251]\n"
    "ds_read_b128 v[92:95], v194 offset:0\n"
    "v_mfma_f32_16x16x32_bf16 v[252:255], v[56:59], v[4:7], v[252:255]\n"
    "ds_read_b128 v[96:99], v194 offset:7168\n"
    "v_mfma_f32_16x16x32_bf16 v[248:251], v[60:63], v[8:11], v[248:251]\n"
    "v_cvt_pk_bf16_f32 v55, v246, v247\n"
    "v_cvt_pk_bf16_f32 v52, v240, v241\n"
    "v_mfma_f32_16x16x32_bf16 v[252:255], v[60:63], v[12:15], v[252:255]\n"
    "v_cvt_pk_bf16_f32 v53, v242, v243\n"
    "v_cvt_pk_bf16_f32 v54, v244, v245\n"
    "v_mfma_f32_16x16x32_bf16 v[248:251], v[64:67], v[16:19], v[248:251]\n"
    "v_xor_b32 v55, v55, v190\n"
    "v_mfma_f32_16x16x32_bf16 v[252:255], v[64:67], v[20:23], v[252:255]\n"
    "buffer_store_dwordx4 v[52:55], v172, s[52:55], s73 offen offset:1024 sc1\n"
    "s_waitcnt lgkmcnt(2)\n"
    "v_mfma_f32_16x16x32_bf16 v[248:251], v[68:71], v[24:27], v[248:251]\n"
    "v_cvt_pk_bf16_f32 v189, v239, v247\n"
    "v_mfma_f32_16x16x32_bf16 v[252:255], v[68:71], v[28:31], v[252:255]\n"
    "buffer_store_dword v189, v176, s[52:55], s73 offen offset:2048 sc1\n"
    "v_mfma_f32_16x16x32_bf16 v[248:251], v[72:75], v[32:35], v[248:251]\n"
    "ds_read_b128 v[100:103], v182 offset:0\n"
    "v_mfma_f32_16x16x32_bf16 v[252:255], v[72:75], v[36:39], v[252:255]\n"
    "ds_read_b128 v[104:107], v182 offset:7168\n"
    "v_mfma_f32_16x16x32_bf16 v[248:251], v[76:79], v[40:43], v[248:251]\n"
    "ds_read_b128 v[108:111], v183 offset:0\n"
    "v_mfma_f32_16x16x32_bf16 v[252:255], v[76:79], v[44:47], v[252:255]\n"
    "ds_read_b128 v[112:115], v183 offset:7168\n"
    "s_waitcnt lgkmcnt(3)\n"
    "v_mfma_f32_16x16x32_bf16 v[200:203], v[92:95], v[0:3], v[200:203]\n"
    "ds_read_b128 v[56:59], v194 offset:1024\n"
    "v_mfma_f32_16x16x32_bf16 v[204:207], v[92:95], v[4:7], v[204:207]\n"
    "ds_read_b128 v[60:63], v194 offset:8192\n"
    "v_mfma_f32_16x16x32_bf16 v[200:203], v[96:99], v[8:11], v[200:203]\n"
    "v_cvt_pk_bf16_f32 v51, v254, v255\n"
    "v_cvt_pk_bf16_f32 v48, v248, v249\n"
    "v_mfma_f32_16x16x32_bf16 v[204:207], v[96:99], v[12:15], v[204:207]\n"
    "v_cvt_pk_bf16_f32 v49, v250, v251\n"
    "v_cvt_pk_bf16_f32 v50, v252, v253\n"
    "v_mfma_f32_16x16x32_bf16 v[200:203], v[100:103], v[16:19], v[200:203]\n"
    "v_xor_b32 v51, v51, v190\n"
    "v_mfma_f32_16x16x32_bf16 v[204:207], v[100:103], v[20:23], v[204:207]\n"
    "buffer_store_dwordx4 v[48:51], v172, s[52:55], s74 offen offset:0 sc1\n"
    "s_waitcnt lgkmcnt(2)\n"
    "v_mfma_f32_16x16x32_bf16 v[200:203], v[104:107], v[24:27], v[200:203]\n"
    "v_cvt_pk_bf16_f32 v188, v255, v255\n"
    "v_mfma_f32_16x16x32_bf16 v[204:207], v[104:107], v[28:31], v[204:207]\n"
    "buffer_store_dword v188, v176, s[52:55], s74 offen offset:2048 sc1\n"
    "v_mfma_f32_16x16x32_bf16 v[200:203], v[108:111], v[32:35], v[200:203]\n"
    "ds_read_b128 v[64:67], v182 offset:1024\n"
    "v_mfma_f32_16x16x32_bf16 v[204:207], v[108:111], v[36:39], v[204:207]\n"
    "ds_read_b128 v[68:71], v182 offset:8192\n"
    "v_mfma_f32_16x16x32_bf16 v[200:203], v[112:115], v[40:43], v[200:203]\n"
    "ds_read_b128 v[72:75], v183 offset:1024\n"
    "v_mfma_f32_16x16x32_bf16 v[204:207], v[112:115], v[44:47], v[204:207]\n"
    "ds_read_b128 v[76:79], v183 offset:8192\n"
    "s_waitcnt lgkmcnt(3)\n"
    "v_mfma_f32_16x16x32_bf16 v[208:211], v[56:59], v[0:3], v[208:211]\n"
    "v_mfma_f32_16x16x32_bf16 v[212:215], v[56:59], v[4:7], v[212:215]\n"
    "v_mfma_f32_16x16x32_bf16 v[208:211], v[60:63], v[8:11], v[208:211]\n"
    "v_lshlrev_b32 v92, 16, v164\n"
    "v_and_b32 v93, 0xffff0000, v164\n"
    "v_mfma_f32_16x16x32_bf16 v[212:215], v[60:63], v[12:15], v[212:215]\n"
    "v_lshlrev_b32 v94, 16, v165\n"
    "v_and_b32 v95, 0xffff0000, v165\n"
    "v_mfma_f32_16x16x32_bf16 v[208:211], v[64:67], v[16:19], v[208:211]\n"
    "v_lshlrev_b32 v96, 16, v168\n"
    "v_and_b32 v97, 0xffff0000, v168\n"
    "v_mfma_f32_16x16x32_bf16 v[212:215], v[64:67], v[20:23], v[212:215]\n"
    "v_lshlrev_b32 v98, 16, v169\n"
    "v_and_b32 v99, 0xffff0000, v169\n"
    "s_waitcnt lgkmcnt(0)\n"
    "v_mfma_f32_16x16x32_bf16 v[208:211], v[68:71], v[24:27], v[208:211]\n"
    "v_lshlrev_b32 v100, 16, v166\n"
    "v_and_b32 v101, 0xffff0000, v166\n"
    "v_mfma_f32_16x16x32_bf16 v[212:215], v[68:71], v[28:31], v[212:215]\n"
    "v_lshlrev_b32 v102, 16, v167\n"
    "v_and_b32 v103, 0xffff0000, v167\n"
    "v_mfma_f32_16x16x32_bf16 v[208:211], v[72:75], v[32:35], v[208:211]\n"
    "v_lshlrev_b32 v104, 16, v170\n"
    "v_and_b32 v105, 0xffff0000, v170\n"
    "v_mfma_f32_16x16x32_bf16 v[212:215], v[72:75], v[36:39], v[212:215]\n"
    "v_lshlrev_b32 v106, 16, v171\n"
    "v_and_b32 v107, 0xffff0000, v171\n"
    "v_mfma_f32_16x16x32_bf16 v[208:211], v[76:79], v[40:43], v[208:211]\n"
    "v_pk_add_f32 v[0:1], v[200:201], v[92:93]\n"
    "v_pk_add_f32 v[2:3], v[202:203], v[94:95]\n"
    "v_mfma_f32_16x16x32_bf16 v[212:215], v[76:79], v[44:47], v[212:215]\n"
    "v_pk_add_f32 v[4:5], v[204:205], v[96:97]\n"
    "v_pk_add_f32 v[6:7], v[206:207], v[98:99]\n"
    "s_nop 1\n"
    "v_pk_add_f32 v[8:9], v[208:209], v[100:101]\n"
    "v_pk_add_f32 v[10:11], v[210:211], v[102:103]\n"
    "s_nop 1\n"
    "v_pk_add_f32 v[12:13], v[212:213], v[104:105]\n"
    "v_pk_add_f32 v[14:15], v[214:215], v[106:107]\n"
    "s_branch .Lrc2_%=\n"
    ".Lct1_%=:\n"
    "s_waitcnt lgkmcnt(0)\n"
    "s_barrier\n"
    "v_mfma_f32_16x16x32_bf16 v[200:203], v[100:103], v[92:95], v[200:203]\n"
    "ds_read_b128 v[0:3], v192 offset:0\n"
    "ds_read_b128 v[56:59], v194 offset:4096\n"
    "v_mfma_f32_16x16x32_bf16 v[204:207], v[100:103], v[96:99], v[204:207]\n"
    "ds_read_b128 v[4:7], v192 offset:512\n"
    "ds_read_b128 v[8:11], v192 offset:2048\n"
    "v_mfma_f32_16x16x32_bf16 v[208:211], v[104:107], v[92:95], v[208:211]\n"
    "ds_read_b128 v[60:63], v194 offset:11264\n"
    "ds_read_b128 v[12:15], v192 offset:2560\n"
    "v_mfma_f32_16x16x32_bf16 v[212:215], v[104:107], v[96:99], v[212:215]\n"
    "ds_read_b128 v[16:19], v180 offset:0\n"
    "ds_read_b128 v[64:67], v182 offset:4096\n"
    "v_mfma_f32_16x16x32_bf16 v[216:219], v[108:111], v[92:95], v[216:219]\n"
    "ds_read_b128 v[20:23], v180 offset:512\n"
    "v_mfma_f32_16x16x32_bf16 v[220:223], v[108:111], v[96:99], v[220:223]\n"
    "ds_read_b128 v[24:27], v180 offset:2048\n"
    "v_mfma_f32_16x16x32_bf16 v[224:227], v[112:115], v[92:95], v[224:227]\n"
    "ds_read_b128 v[68:71], v182 offset:11264\n"
    "v_mfma_f32_16x16x32_bf16 v[228:231], v[112:115], v[96:99], v[228:231]\n"
    "ds_read_b128 v[28:31], v180 offset:2560\n"
    "v_mfma_f32_16x16x32_bf16 v[232:235], v[116:119], v[92:95], v[232:235]\n"
    "ds_read_b128 v[32:35], v181 offset:0\n"
    "v_mfma_f32_16x16x32_bf16 v[236:239], v[116:119], v[96:99], v[236:239]\n"
    "ds_read_b128 v[72:75], v183 offset:4096\n"
    "v_mfma_f32_16x16x32_bf16 v[240:243], v[120:123], v[92:95], v[240:243]\n"
    "ds_read_b128 v[36:39], v181 offset:512\n"
    "v_mfma_f32_16x16x32_bf16 v[244:247], v[120:123], v[96:99], v[244:247]\n"
    "s_waitcnt lgkmcnt(9)\n"
    "ds_read_b128 v[40:43], v181 offset:2048\n"
    "v_mfma_f32_16x16x32_bf16 v[248:251], v[124:127], v[92:95], v[248:251]\n"
    "ds_read_b128 v[76:79], v183 offset:11264\n"
    "v_mfma_f32_16x16x32_bf16 v[252:255], v[124:127], v[96:99], v[252:255]\n"
    "ds_read_b128 v[44:47], v181 offset:2560\n"
    "v_mfma_f32_16x16x32_bf16 v[232:235], v[56:59], v[0:3], v[232:235]\n"
    "ds_read_b128 v[92:95], v194 offset:5120\n"
    "v_mfma_f32_16x16x32_bf16 v[236:239], v[56:59], v[4:7], v[236:239]\n"
    "ds_read_b128 v[96:99], v194 offset:12288\n"
    "v_mfma_f32_16x16x32_bf16 v[232:235], v[60:63], v[8:11], v[232:235]\n"
    "ds_read_b128 v[100:103], v182 offset:5120\n"
    "s_waitcnt lgkmcnt(9)\n"
    "ds_read_b128 v[104:107], v182 offset:12288\n"
    "v_mfma_f32_16x16x32_bf16 v[236:239], v[60:63], v[12:15], v[236:239]\n"
    "ds_read_b128 v[108:111], v183 offset:5120\n"
    "ds_read_b128 v[112:115], v183 offset:12288\n"
    "v_mfma_f32_16x16x32_bf16 v[232:235], v[64:67], v[16:19], v[232:235]\n"
    "v_mfma_f32_16x16x32_bf16 v[236:239], v[64:67], v[20:23], v[236:239]\n"
    "v_mfma_f32_16x16x32_bf16 v[232:235], v[68:71], v[24:27], v[232:235]\n"
    "v_mfma_f32_16x16x32_bf16 v[236:239], v[68:71], v[28:31], v[236:239]\n"
    "s_waitcnt lgkmcnt(6)\n"
    "v_mfma_f32_16x16x32_bf16 v[232:235], v[72:75], v[32:35], v[232:235]\n"
    "v_mfma_f32_16x16x32_bf16 v[236:239], v[72:75], v[36:39], v[236:239]\n"
    "v_mfma_f32_16x16x32_bf16 v[232:235], v[76:79], v[40:43], v[232:235]\n"
    "v_mfma_f32_16x16x32_bf16 v[236:239], v[76:79], v[44:47], v[236:239]\n"
    "s_waitcnt lgkmcnt(3)\n"
    "v_mfma_f32_16x16x32_bf16 v[240:243], v[92:95], v[0:3], v[240:243]\n"
    "ds_read_b128 v[56:59], v194 offset:6144\n"
    "v_mfma_f32_16x16x32_bf16 v[244:247], v[92:95], v[4:7], v[244:247]\n"
    "ds_read_b128 v[60:63], v194 offset:13312\n"
    "v_mfma_f32_16x16x32_bf16 v[240:243], v[96:99], v[8:11], v[240:243]\n"
    "v_cvt_pk_bf16_f32 v51, v238, v239\n"
    "v_cvt_pk_bf16_f32 v48, v232, v233\n"
    "v_mfma_f32_16x16x32_bf16 v[244:247], v[96:99], v[12:15], v[244:247]\n"
    "v_cvt_pk_bf16_f32 v49, v234, v235\n"
    "v_cvt_pk_bf16_f32 v50, v236, v237\n"
    "v_mfma_f32_16x16x32_bf16 v[240:243], v[100:103], v[16:19], v[240:243]\n"
    "v_xor_b32 v51, v51, v190\n"
    "v_mfma_f32_16x16x32_bf16 v[244:247], v[100:103], v[20:23], v[244:247]\n"
    "buffer_store_dwordx4 v[48:51], v172, s[52:55], s72 offen offset:0 sc1\n"
    "s_waitcnt lgkmcnt(2)\n"
    "v_mfma_f32_16x16x32_bf16 v[240:243], v[104:107], v[24:27], v[240:243]\n"
    "ds_read_b128 v[64:67], v182 offset:6144\n"
    "v_mfma_f32_16x16x32_bf16 v[244:247], v[104:107], v[28:31], v[244:247]\n"
    "ds_read_b128 v[68:71], v182 offset:13312\n"
    "v_mfma_f32_16x16x32_bf16 v[240:243], v[108:111], v[32:35], v[240:243]\n"
    "ds_read_b128 v[72:75], v183 offset:6144\n"
    "v_mfma_f32_16x16x32_bf16 v[244:247], v[108:111], v[36:39], v[244:247]\n"
    "ds_read_b128 v[76:79], v183 offset:13312\n"
    "v_mfma_f32_16x16x32_bf16 v[240:243], v[112:115], v[40:43], v[240:243]\n"
    "v_mfma_f32_16x16x32_bf16 v[244:247], v[112:115], v[44:47], v[244:247]\n"
    "s_waitcnt lgkmcnt(3)\n"
    "v_mfma_f32_16x16x32_bf16 v[248:251], v[56:59], v[0:3], v[248:251]\n"
    "ds_read_b128 v[92:95], v194 offset:0\n"
    "v_mfma_f32_16x16x32_bf16 v[252:255], v[56:59], v[4:7], v[252:255]\n"
    "ds_read_b128 v[96:99], v194 offset:7168\n"
    "v_mfma_f32_16x16x32_bf16 v[248:251], v[60:63], v[8:11], v[248:251]\n"
    "v_cvt_pk_bf16_f32 v55, v246, v247\n"
    "v_cvt_pk_bf16_f32 v52, v240, v241\n"
    "v_mfma_f32_16x16x32_bf16 v[252:255], v[60:63], v[12:15], v[252:255]\n"
    "v_cvt_pk_bf16_f32 v53, v242, v243\n"
    "v_cvt_pk_bf16_f32 v54, v244, v245\n"
    "v_mfma_f32_16x16x32_bf16 v[248:251], v[64:67], v[16:19], v[248:251]\n"
    "v_xor_b32 v55, v55, v190\n"
    "v_mfma_f32_16x16x32_bf16 v[252:255], v[64:67], v[20:23], v[252:255]\n"
    "buffer_store_dwordx4 v[52:55], v172, s[52:55], s72 offen offset:1024 sc1\n"
    "s_waitcnt lgkmcnt(2)\n"
    "v_mfma_f32_16x16x32_bf16 v[248:251], v[68:71], v[24:27], v[248:251]\n"
    "v_cvt_pk_bf16_f32 v188, v239, v247\n"
    "v_mfma_f32_16x16x32_bf16 v[252:255], v[68:71], v[28:31], v[252:255]\n"
    "buffer_store_dword v188, v176, s[52:55], s72 offen offset:2048 sc1\n"
    "v_mfma_f32_16x16x32_bf16 v[248:251], v[72:75], v[32:35], v[248:251]\n"
    "ds_read_b128 v[100:103], v182 offset:0\n"
    "v_mfma_f32_16x16x32_bf16 v[252:255], v[72:75], v[36:39], v[252:255]\n"
    "ds_read_b128 v[104:107], v182 offset:7168\n"
    "v_mfma_f32_16x16x32_bf16 v[248:251], v[76:79], v[40:43], v[248:251]\n"
    "ds_read_b128 v[108:111], v183 offset:0\n"
    "v_mfma_f32_16x16x32_bf16 v[252:255], v[76:79], v[44:47], v[252:255]\n"
    "ds_read_b128 v[112:115], v183 offset:7168\n"
    "s_waitcnt lgkmcnt(3)\n"
    "v_mfma_f32_16x16x32_bf16 v[200:203], v[92:95], v[0:3], v[200:203]\n"
    "ds_read_b128 v[56:59], v194 offset:1024\n"
    "v_mfma_f32_16x16x32_bf16 v[204:207], v[92:95], v[4:7], v[204:207]\n"
    "ds_read_b128 v[60:63], v194 offset:8192\n"
    "v_mfma_f32_16x16x32_bf16 v[200:203], v[96:99], v[8:11], v[200:203]\n"
    "v_cvt_pk_bf16_f32 v51, v254, v255\n"
    "v_cvt_pk_bf16_f32 v48, v248, v249\n"
    "v_mfma_f32_16x16x32_bf16 v[204:207], v[96:99], v[12:15], v[204:207]\n"
    "v_cvt_pk_bf16_f32 v49, v250, v251\n"
    "v_cvt_pk_bf16_f32 v50, v252, v253\n"
    "v_mfma_f32_16x16x32_bf16 v[200:203], v[100:103], v[16:19], v[200:203]\n"
    "v_xor_b32 v51, v51, v190\n"
    "v_mfma_f32_16x16x32_bf16 v[204:207], v[100:103], v[20:23], v[204:207]\n"
    "buffer_store_dwordx4 v[48:51], v172, s[52:55], s73 offen offset:0 sc1\n"
    "s_waitcnt lgkmcnt(2)\n"
    "v_mfma_f32_16x16x32_bf16 v[200:203], v[104:107], v[24:27], v[200:203]\n"
    "v_cvt_pk_bf16_f32 v189, v255, v255\n"
    "v_mfma_f32_16x16x32_bf16 v[204:207], v[104:107], v[28:31], v[204:207]\n"
    "buffer_store_dword v189, v176, s[52:55], s73 offen offset:2048 sc1\n"
    "v_mfma_f32_16x16x32_bf16 v[200:203], v[108:111], v[32:35], v[200:203]\n"
    "ds_read_b128 v[64:67], v182 offset:1024\n"
    "v_mfma_f32_16x16x32_bf16 v[204:207], v[108:111], v[36:39], v[204:207]\n"
    "ds_read_b128 v[68:71], v182 offset:8192\n"
    "v_mfma_f32_16x16x32_bf16 v[200:203], v[112:115], v[40:43], v[200:203]\n"
    "ds_read_b128 v[72:75], v183 offset:1024\n"
    "v_mfma_f32_16x16x32_bf16 v[204:207], v[112:115], v[44:47], v[204:207]\n"
    "ds_read_b128 v[76:79], v183 offset:8192\n"
    "s_waitcnt lgkmcnt(3)\n"
    "v_mfma_f32_16x16x32_bf16 v[208:211], v[56:59], v[0:3], v[208:211]\n"
    "ds_read_b128 v[92:95], v194 offset:2048\n"
    "v_mfma_f32_16x16x32_bf16 v[212:215], v[56:59], v[4:7], v[212:215]\n"
    "ds_read_b128 v[96:99], v194 offset:9216\n"
    "v_mfma_f32_16x16x32_bf16 v[208:211], v[60:63], v[8:11], v[208:211]\n"
    "v_cvt_pk_bf16_f32 v55, v206, v207\n"
    "v_cvt_pk_bf16_f32 v52, v200, v201\n"
    "v_mfma_f32_16x16x32_bf16 v[212:215], v[60:63], v[12:15], v[212:215]\n"
    "v_cvt_pk_bf16_f32 v53, v202, v203\n"
    "v_cvt_pk_bf16_f32 v54, v204, v205\n"
    "v_mfma_f32_16x16x32_bf16 v[208:211], v[64:67], v[16:19], v[208:211]\n"
    "v_xor_b32 v55, v55, v190\n"
    "v_mfma_f32_16x16x32_bf16 v[212:215], v[64:67], v[20:23], v[212:215]\n"
    "buffer_store_dwordx4 v[52:55], v172, s[52:55], s74 offen offset:0 sc1\n"
    "s_waitcnt lgkmcnt(2)\n"
    "v_mfma_f32_16x16x32_bf16 v[208:211], v[68:71], v[24:27], v[208:211]\n"
    "ds_read_b128 v[100:103], v182 offset:2048\n"
    "v_mfma_f32_16x16x32_bf16 v[212:215], v[68:71], v[28:31], v[212:215]\n"
    "ds_read_b128 v[104:107], v182 offset:9216\n"
    "v_mfma_f32_16x16x32_bf16 v[208:211], v[72:75], v[32:35], v[208:211]\n"
    "ds_read_b128 v[108:111], v183 offset:2048\n"
    "v_mfma_f32_16x16x32_bf16 v[212:215], v[72:75], v[36:39], v[212:215]\n"
    "ds_read_b128 v[112:115], v183 offset:9216\n"
    "v_mfma_f32_16x16x32_bf16 v[208:211], v[76:79], v[40:43], v[208:211]\n"
    "v_mfma_f32_16x16x32_bf16 v[212:215], v[76:79], v[44:47], v[212:215]\n"
    "s_waitcnt lgkmcnt(3)\n"
    "v_mfma_f32_16x16x32_bf16 v[216:219], v[92:95], v[0:3], v[216:219]\n"
    "ds_read_b128 v[56:59], v194 offset:3072\n"
    "v_mfma_f32_16x16x32_bf16 v[220:223], v[92:95], v[4:7], v[220:223]\n"
    "ds_read_b128 v[60:63], v194 offset:10240\n"
    "v_mfma_f32_16x16x32_bf16 v[216:219], v[96:99], v[8:11], v[216:219]\n"
    "v_cvt_pk_bf16_f32 v51, v214, v215\n"
    "v_cvt_pk_bf16_f32 v48, v208, v209\n"
    "v_mfma_f32_16x16x32_bf16 v[220:223], v[96:99], v[12:15], v[220:223]\n"
    "v_cvt_pk_bf16_f32 v49, v210, v211\n"
    "v_cvt_pk_bf16_f32 v50, v212, v213\n"
    "v_mfma_f32_16x16x32_bf16 v[216:219], v[100:103], v[16:19], v[216:219]\n"
    "v_xor_b32 v51, v51, v190\n"
    "v_mfma_f32_16x16x32_bf16 v[220:223], v[100:103], v[20:23], v[220:223]\n"
    "buffer_store_dwordx4 v[48:51], v172, s[52:55], s74 offen offset:1024 sc1\n"
    "s_waitcnt lgkmcnt(2)\n"
    "v_mfma_f32_16x16x32_bf16 v[216:219], v[104:107], v[24:27], v[216:219]\n"
    "v_cvt_pk_bf16_f32 v188, v207, v215\n"
    "v_mfma_f32_16x16x32_bf16 v[220:223], v[104:107], v[28:31], v[220:223]\n"
    "buffer_store_dword v188, v176, s[52:55], s74 offen offset:2048 sc1\n"
    "v_mfma_f32_16x16x32_bf16 v[216:219], v[108:111], v[32:35], v[216:219]\n"
    "ds_read_b128 v[64:67], v182 offset:3072\n"
    "v_mfma_f32_16x16x32_bf16 v[220:223], v[108:111], v[36:39], v[220:223]\n"
    "ds_read_b128 v[68:71], v182 offset:10240\n"
    "v_mfma_f32_16x16x32_bf16 v[216:219], v[112:115], v[40:43], v[216:219]\n"
    "ds_read_b128 v[72:75], v183 offset:3072\n"
    "v_mfma_f32_16x16x32_bf16 v[220:223], v[112:115], v[44:47], v[220:223]\n"
    "ds_read_b128 v[76:79], v183 offset:10240\n"
    "s_waitcnt lgkmcnt(3)\n"
    "v_mfma_f32_16x16x32_bf16 v[224:227], v[56:59], v[0:3], v[224:227]\n"
    "v_mfma_f32_16x16x32_bf16 v[228:231], v[56:59], v[4:7], v[228:231]\n"
    "v_mfma_f32_16x16x32_bf16 v[224:227], v[60:63], v[8:11], v[224:227]\n"
    "v_lshlrev_b32 v92, 16, v164\n"
    "v_and_b32 v93, 0xffff0000, v164\n"
    "v_mfma_f32_16x16x32_bf16 v[228:231], v[60:63], v[12:15], v[228:231]\n"
    "v_lshlrev_b32 v94, 16, v165\n"
    "v_and_b32 v95, 0xffff0000, v165\n"
    "v_mfma_f32_16x16x32_bf16 v[224:227], v[64:67], v[16:19], v[224:227]\n"
    "v_lshlrev_b32 v96, 16, v168\n"
    "v_and_b32 v97, 0xffff0000, v168\n"
    "v_mfma_f32_16x16x32_bf16 v[228:231], v[64:67], v[20:23], v[228:231]\n"
    "v_lshlrev_b32 v98, 16, v169\n"
    "v_and_b32 v99, 0xffff0000, v169\n"
    "s_waitcnt lgkmcnt(0)\n"
    "v_mfma_f32_16x16x32_bf16 v[224:227], v[68:71], v[24:27], v[224:227]\n"
    "v_lshlrev_b32 v100, 16, v166\n"
    "v_and_b32 v101, 0xffff0000, v166\n"
    "v_mfma_f32_16x16x32_bf16 v[228:231], v[68:71], v[28:31], v[228:231]\n"
    "v_lshlrev_b32 v102, 16, v167\n"
    "v_and_b32 v103, 0xffff0000, v167\n"
    "v_mfma_f32_16x16x32_bf16 v[224:227], v[72:75], v[32:35], v[224:227]\n"
    "v_lshlrev_b32 v104, 16, v170\n"
    "v_and_b32 v105, 0xffff0000, v170\n"
    "v_mfma_f32_16x16x32_bf16 v[228:231], v[72:75], v[36:39], v[228:231]\n"
    "v_lshlrev_b32 v106, 16, v171\n"
    "v_and_b32 v107, 0xffff0000, v171\n"
    "v_mfma_f32_16x16x32_bf16 v[224:227], v[76:79], v[40:43], v[224:227]\n"
    "v_pk_add_f32 v[0:1], v[216:217], v[92:93]\n"
    "v_pk_add_f32 v[2:3], v[218:219], v[94:95]\n"
    "v_mfma_f32_16x16x32_bf16 v[228:231], v[76:79], v[44:47], v[228:231]\n"
    "v_pk_add_f32 v[4:5], v[220:221], v[96:97]\n"
    "v_pk_add_f32 v[6:7], v[222:223], v[98:99]\n"
    "s_nop 1\n"
    "v_pk_add_f32 v[8:9], v[224:225], v[100:101]\n"
    "v_pk_add_f32 v[10:11], v[226:227], v[102:103]\n"
    "s_nop 1\n"
    "v_pk_add_f32 v[12:13], v[228:229], v[104:105]\n"
    "v_pk_add_f32 v[14:15], v[230:231], v[106:107]\n"
    "s_branch .Lrc2_%=\n"
    ".Lct2_%=:\n"
    "s_waitcnt lgkmcnt(0)\n"
    "s_barrier\n"
    "v_mfma_f32_16x16x32_bf16 v[200:203], v[100:103], v[92:95], v[200:203]\n"
    "ds_read_b128 v[0:3], v192 offset:0\n"
    "ds_read_b128 v[56:59], v194 offset:6144\n"
    "v_mfma_f32_16x16x32_bf16 v[204:207], v[100:103], v[96:99], v[204:207]\n"
    "ds_read_b128 v[4:7], v192 offset:512\n"
    "ds_read_b128 v[8:11], v192 offset:2048\n"
    "v_mfma_f32_16x16x32_bf16 v[208:211], v[104:107], v[92:95], v[208:211]\n"
    "ds_read_b128 v[60:63], v194 offset:13312\n"
    "ds_read_b128 v[12:15], v192 offset:2560\n"
    "v_mfma_f32_16x16x32_bf16 v[212:215], v[104:107], v[96:99], v[212:215]\n"
    "ds_read_b128 v[16:19], v180 offset:0\n"
    "ds_read_b128 v[64:67], v182 offset:6144\n"
    "v_mfma_f32_16x16x32_bf16 v[216:219], v[108:111], v[92:95], v[216:219]\n"
    "ds_read_b128 v[20:23], v180 offset:512\n"
    "v_mfma_f32_16x16x32_bf16 v[220:223], v[108:111], v[96:99], v[220:223]\n"
    "ds_read_b128 v[24:27], v180 offset:2048\n"
    "v_mfma_f32_16x16x32_bf16 v[224:227], v[112:115], v[92:95], v[224:227]\n"
    "ds_read_b128 v[68:71], v182 offset:13312\n"
    "v_mfma_f32_16x16x32_bf16 v[228:231], v[112:115], v[96:99], v[228:231]\n"
    "ds_read_b128 v[28:31], v180 offset:2560\n"
    "v_mfma_f32_16x16x32_bf16 v[232:235], v[116:119], v[92:95], v[232:235]\n"
    "ds_read_b128 v[32:35], v181 offset:0\n"
    "v_mfma_f32_16x16x32_bf16 v[236:239], v[116:119], v[96:99], v[236:239]\n"
    "ds_read_b128 v[72:75], v183 offset:6144\n"
    "v_mfma_f32_16x16x32_bf16 v[240:243], v[120:123], v[92:95], v[240:243]\n"
    "ds_read_b128 v[36:39], v181 offset:512\n"
    "v_mfma_f32_16x16x32_bf16 v[244:247], v[120:123], v[96:99], v[244:247]\n"
    "s_waitcnt lgkmcnt(9)\n"
    "ds_read_b128 v[40:43], v181 offset:2048\n"
    "v_mfma_f32_16x16x32_bf16 v[248:251], v[124:127], v[92:95], v[248:251]\n"
    "ds_read_b128 v[76:79], v183 offset:13312\n"
    "v_mfma_f32_16x16x32_bf16 v[252:255], v[124:127], v[96:99], v[252:255]\n"
    "ds_read_b128 v[44:47], v181 offset:2560\n"
    "v_mfma_f32_16x16x32_bf16 v[248:251], v[56:59], v[0:3], v[248:251]\n"
    "ds_read_b128 v[92:95], v194 offset:0\n"
    "v_mfma_f32_16x16x32_bf16 v[252:255], v[56:59], v[4:7], v[252:255]\n"
    "ds_read_b128 v[96:99], v194 offset:7168\n"
    "v_mfma_f32_16x16x32_bf16 v[248:251], v[60:63], v[8:11], v[248:251]\n"
    "ds_read_b128 v[100:103], v182 offset:0\n"
    "s_waitcnt lgkmcnt(9)\n"
    "ds_read_b128 v[104:107], v182 offset:7168\n"
    "v_mfma_f32_16x16x32_bf16 v[252:255], v[60:63], v[12:15], v[252:255]\n"
    "ds_read_b128 v[108:111], v183 offset:0\n"
    "ds_read_b128 v[112:115], v183 offset:7168\n"
    "v_mfma_f32_16x16x32_bf16 v[248:251], v[64:67], v[16:19], v[248:251]\n"
    "v_mfma_f32_16x16x32_bf16 v[252:255], v[64:67], v[20:23], v[252:255]\n"
    "v_mfma_f32_16x16x32_bf16 v[248:251], v[68:71], v[24:27], v[248:251]\n"
    "v_mfma_f32_16x16x32_bf16 v[252:255], v[68:71], v[28:31], v[252:255]\n"
    "s_waitcnt lgkmcnt(6)\n"
    "v_mfma_f32_16x16x32_bf16 v[248:251], v[72:75], v[32:35], v[248:251]\n"
    "v_mfma_f32_16x16x32_bf16 v[252:255], v[72:75], v[36:39], v[252:255]\n"
    "v_mfma_f32_16x16x32_bf16 v[248:251], v[76:79], v[40:43], v[248:251]\n"
    "v_mfma_f32_16x16x32_bf16 v[252:255], v[76:79], v[44:47], v[252:255]\n"
    "s_waitcnt lgkmcnt(3)\n"
    "v_mfma_f32_16x16x32_bf16 v[200:203], v[92:95], v[0:3], v[200:203]\n"
    "ds_read_b128 v[56:59], v194 offset:1024\n"
    "v_mfma_f32_16x16x32_bf16 v[204:207], v[92:95], v[4:7], v[204:207]\n"
    "ds_read_b128 v[60:63], v194 offset:8192\n"
    "v_mfma_f32_16x16x32_bf16 v[200:203], v[96:99], v[8:11], v[200:203]\n"
    "v_cvt_pk_bf16_f32 v51, v254, v255\n"
    "v_cvt_pk_bf16_f32 v48, v248, v249\n"
    "v_mfma_f32_16x16x32_bf16 v[204:207], v[96:99], v[12:15], v[204:207]\n"
    "v_cvt_pk_bf16_f32 v49, v250, v251\n"
    "v_cvt_pk_bf16_f32 v50, v252, v253\n"
    "v_mfma_f32_16x16x32_bf16 v[200:203], v[100:103], v[16:19], v[200:203]\n"
    "v_xor_b32 v51, v51, v190\n"
    "v_mfma_f32_16x16x32_bf16 v[204:207], v[100:103], v[20:23], v[204:207]\n"
    "buffer_store_dwordx4 v[48:51], v172, s[52:55], s72 offen offset:0 sc1\n"
    "s_waitcnt lgkmcnt(2)\n"
    "v_mfma_f32_16x16x32_bf16 v[200:203], v[104:107], v[24:27], v[200:203]\n"
    "v_cvt_pk_bf16_f32 v188, v255, v255\n"
    "v_mfma_f32_16x16x32_bf16 v[204:207], v[104:107], v[28:31], v[204:207]\n"
    "buffer_store_dword v188, v176, s[52:55], s72 offen offset:2048 sc1\n"
    "v_mfma_f32_16x16x32_bf16 v[200:203], v[108:111], v[32:35], v[200:203]\n"
    "ds_read_b128 v[64:67], v182 offset:1024\n"
    "v_mfma_f32_16x16x32_bf16 v[204:207], v[108:111], v[36:39], v[204:207]\n"
    "ds_read_b128 v[68:71], v182 offset:8192\n"
    "v_mfma_f32_16x16x32_bf16 v[200:203], v[112:115], v[40:43], v[200:203]\n"
    "ds_read_b128 v[72:75], v183 offset:1024\n"
    "v_mfma_f32_16x16x32_bf16 v[204:207], v[112:115], v[44:47], v[204:207]\n"
    "ds_read_b128 v[76:79], v183 offset:8192\n"
    "s_waitcnt lgkmcnt(3)\n"
    "v_mfma_f32_16x16x32_bf16 v[208:211], v[56:59], v[0:3], v[208:211]\n"
    "ds_read_b128 v[92:95], v194 offset:2048\n"
    "v_mfma_f32_16x16x32_bf16 v[212:215], v[56:59], v[4:7], v[212:215]\n"
    "ds_read_b128 v[96:99], v194 offset:9216\n"
    "v_mfma_f32_16x16x32_bf16 v[208:211], v[60:63], v[8:11], v[208:211]\n"
    "v_cvt_pk_bf16_f32 v55, v206, v207\n"
    "v_cvt_pk_bf16_f32 v52, v200, v201\n"
    "v_mfma_f32_16x16x32_bf16 v[212:215], v[60:63], v[12:15], v[212:215]\n"
    "v_cvt_pk_bf16_f32 v53, v202, v203\n"
    "v_cvt_pk_bf16_f32 v54, v204, v205\n"
    "v_mfma_f32_16x16x32_bf16 v[208:211], v[64:67], v[16:19], v[208:211]\n"
    "v_xor_b32 v55, v55, v190\n"
    "v_mfma_f32_16x16x32_bf16 v[212:215], v[64:67], v[20:23], v[212:215]\n"
    "buffer_store_dwordx4 v[52:55], v172, s[52:55], s73 offen offset:0 sc1\n"
    "s_waitcnt lgkmcnt(2)\n"
    "v_mfma_f32_16x16x32_bf16 v[208:211], v[68:71], v[24:27], v[208:211]\n"
    "ds_read_b128 v[100:103], v182 offset:2048\n"
    "v_mfma_f32_16x16x32_bf16 v[212:215], v[68:71], v[28:31], v[212:215]\n"
    "ds_read_b128 v[104:107], v182 offset:9216\n"
    "v_mfma_f32_16x16x32_bf16 v[208:211], v[72:75], v[32:35], v[208:211]\n"
    "ds_read_b128 v[108:111], v183 offset:2048\n"
    "v_mfma_f32_16x16x32_bf16 v[212:215], v[72:75], v[36:39], v[212:215]\n"
    "ds_read_b128 v[112:115], v183 offset:9216\n"
    "v_mfma_f32_16x16x32_bf16 v[208:211], v[76:79], v[40:43], v[208:211]\n"
    "v_mfma_f32_16x16x32_bf16 v[212:215], v[76:79], v[44:47], v[212:215]\n"
    "s_waitcnt lgkmcnt(3)\n"
    "v_mfma_f32_16x16x32_bf16 v[216:219], v[92:95], v[0:3], v[216:219]\n"
    "ds_read_b128 v[56:59], v194 offset:3072\n"
    "v_mfma_f32_16x16x32_bf16 v[220:223], v[92:95], v[4:7], v[220:223]\n"
    "ds_read_b128 v[60:63], v194 offset:10240\n"
    "v_mfma_f32_16x16x32_bf16 v[216:219], v[96:99], v[8:11], v[216:219]\n"
    "v_cvt_pk_bf16_f32 v51, v214, v215\n"
    "v_cvt_pk_bf16_f32 v48, v208, v209\n"
    "v_mfma_f32_16x16x32_bf16 v[220:223], v[96:99], v[12:15], v[220:223]\n"
    "v_cvt_pk_bf16_f32 v49, v210, v211\n"
    "v_cvt_pk_bf16_f32 v50, v212, v213\n"
    "v_mfma_f32_16x16x32_bf16 v[216:219], v[100:103], v[16:19], v[216:219]\n"
    "v_xor_b32 v51, v51, v190\n"
    "v_mfma_f32_16x16x32_bf16 v[220:223], v[100:103], v[20:23], v[220:223]\n"
    "buffer_store_dwordx4 v[48:51], v172, s[52:55], s73 offen offset:1024 sc1\n"
    "s_waitcnt lgkmcnt(2)\n"
    "v_mfma_f32_16x16x32_bf16 v[216:219], v[104:107], v[24:27], v[216:219]\n"
    "v_cvt_pk_bf16_f32 v189, v207, v215\n"
    "v_mfma_f32_16x16x32_bf16 v[220:223], v[104:107], v[28:31], v[220:223]\n"
    "buffer_store_dword v189, v176, s[52:55], s73 offen offset:2048 sc1\n"
    "v_mfma_f32_16x16x32_bf16 v[216:219], v[108:111], v[32:35], v[216:219]\n"
    "ds_read_b128 v[64:67], v182 offset:3072\n"
    "v_mfma_f32_16x16x32_bf16 v[220:223], v[108:111], v[36:39], v[220:223]\n"
    "ds_read_b128 v[68:71], v182 offset:10240\n"
    "v_mfma_f32_16x16x32_bf16 v[216:219], v[112:115], v[40:43], v[216:219]\n"
    "ds_read_b128 v[72:75], v183 offset:3072\n"
    "v_mfma_f32_16x16x32_bf16 v[220:223], v[112:115], v[44:47], v[220:223]\n"
    "ds_read_b128 v[76:79], v183 offset:10240\n"
    "s_waitcnt lgkmcnt(3)\n"
    "v_mfma_f32_16x16x32_bf16 v[224:227], v[56:59], v[0:3], v[224:227]\n"
    "ds_read_b128 v[92:95], v194 offset:4096\n"
    "v_mfma_f32_16x16x32_bf16 v[228:231], v[56:59], v[4:7], v[228:231]\n"
    "ds_read_b128 v[96:99], v194 offset:11264\n"
    "v_mfma_f32_16x16x32_bf16 v[224:227], v[60:63], v[8:11], v[224:227]\n"
    "v_cvt_pk_bf16_f32 v55, v222, v223\n"
    "v_cvt_pk_bf16_f32 v52, v216, v217\n"
    "v_mfma_f32_16x16x32_bf16 v[228:231], v[60:63], v[12:15], v[228:231]\n"
    "v_cvt_pk_bf16_f32 v53, v218, v219\n"
    "v_cvt_pk_bf16_f32 v54, v220, v221\n"
    "v_mfma_f32_16x16x32_bf16 v[224:227], v[64:67], v[16:19], v[224:227]\n"
    "v_xor_b32 v55, v55, v190\n"
    "v_mfma_f32_16x16x32_bf16 v[228:231], v[64:67], v[20:23], v[228:231]\n"
    "buffer_store_dwordx4 v[52:55], v172, s[52:55], s74 offen offset:0 sc1\n"
    "s_waitcnt lgkmcnt(2)\n"
    "v_mfma_f32_16x16x32_bf16 v[224:227], v[68:71], v[24:27], v[224:227]\n"
    "ds_read_b128 v[100:103], v182 offset:4096\n"
    "v_mfma_f32_16x16x32_bf16 v[228:231], v[68:71], v[28:31], v[228:231]\n"
    "ds_read_b128 v[104:107], v182 offset:11264\n"
    "v_mfma_f32_16x16x32_bf16 v[224:227], v[72:75], v[32:35], v[224:227]\n"
    "ds_read_b128 v[108:111], v183 offset:4096\n"
    "v_mfma_f32_16x16x32_bf16 v[228:231], v[72:75], v[36:39], v[228:231]\n"
    "ds_read_b128 v[112:115], v183 offset:11264\n"
    "v_mfma_f32_16x16x32_bf16 v[224:227], v[76:79], v[40:43], v[224:227]\n"
    "v_mfma_f32_16x16x32_bf16 v[228:231], v[76:79], v[44:47], v[228:231]\n"
    "s_waitcnt lgkmcnt(3)\n"
    "v_mfma_f32_16x16x32_bf16 v[232:235], v[92:95], v[0:3], v[232:235]\n"
    "ds_read_b128 v[56:59], v194 offset:5120\n"
    "v_mfma_f32_16x16x32_bf16 v[236:239], v[92:95], v[4:7], v[236:239]\n"
    "ds_read_b128 v[60:63], v194 offset:12288\n"
    "v_mfma_f32_16x16x32_bf16 v[232:235], v[96:99], v[8:11], v[232:235]\n"
    "v_cvt_pk_bf16_f32 v51, v230, v231\n"
    "v_cvt_pk_bf16_f32 v48, v224, v225\n"
    "v_mfma_f32_16x16x32_bf16 v[236:239], v[96:99], v[12:15], v[236:239]\n"
    "v_cvt_pk_bf16_f32 v49, v226, v227\n"
    "v_cvt_pk_bf16_f32 v50, v228, v229\n"
    "v_mfma_f32_16x16x32_bf16 v[232:235], v[100:103], v[16:19], v[232:235]\n"
    "v_xor_b32 v51, v51, v190\n"
    "v_mfma_f32_16x16x32_bf16 v[236:239], v[100:103], v[20:23], v[236:239]\n"
    "buffer_store_dwordx4 v[48:51], v172, s[52:55], s74 offen offset:1024 sc1\n"
    "s_waitcnt lgkmcnt(2)\n"
    "v_mfma_f32_16x16x32_bf16 v[232:235], v[104:107], v[24:27], v[232:235]\n"
    "v_cvt_pk_bf16_f32 v188, v223, v231\n"
    "v_mfma_f32_16x16x32_bf16 v[236:239], v[104:107], v[28:31], v[236:239]\n"
    "buffer_store_dword v188, v176, s[52:55], s74 offen offset:2048 sc1\n"
    "v_mfma_f32_16x16x32_bf16 v[232:235], v[108:111], v[32:35], v[232:235]\n"
    "ds_read_b128 v[64:67], v182 offset:5120\n"
    "v_mfma_f32_16x16x32_bf16 v[236:239], v[108:111], v[36:39], v[236:239]\n"
    "ds_read_b128 v[68:71], v182 offset:12288\n"
    "v_mfma_f32_16x16x32_bf16 v[232:235], v[112:115], v[40:43], v[232:235]\n"
    "ds_read_b128 v[72:75], v183 offset:5120\n"
    "v_mfma_f32_16x16x32_bf16 v[236:239], v[112:115], v[44:47], v[236:239]\n"
    "ds_read_b128 v[76:79], v183 offset:12288\n"
    "s_waitcnt lgkmcnt(3)\n"
    "v_mfma_f32_16x16x32_bf16 v[240:243], v[56:59], v[0:3], v[240:243]\n"
    "v_mfma_f32_16x16x32_bf16 v[244:247], v[56:59], v[4:7], v[244:247]\n"
    "v_mfma_f32_16x16x32_bf16 v[240:243], v[60:63], v[8:11], v[240:243]\n"
    "v_lshlrev_b32 v92, 16, v164\n"
    "v_and_b32 v93, 0xffff0000, v164\n"
    "v_mfma_f32_16x16x32_bf16 v[244:247], v[60:63], v[12:15], v[244:247]\n"
    "v_lshlrev_b32 v94, 16, v165\n"
    "v_and_b32 v95, 0xffff0000, v165\n"
    "v_mfma_f32_16x16x32_bf16 v[240:243], v[64:67], v[16:19], v[240:243]\n"
    "v_lshlrev_b32 v96, 16, v168\n"
    "v_and_b32 v97, 0xffff0000, v168\n"
    "v_mfma_f32_16x16x32_bf16 v[244:247], v[64:67], v[20:23], v[244:247]\n"
    "v_lshlrev_b32 v98, 16, v169\n"
    "v_and_b32 v99, 0xffff0000, v169\n"
    "s_waitcnt lgkmcnt(0)\n"
    "v_mfma_f32_16x16x32_bf16 v[240:243], v[68:71], v[24:27], v[240:243]\n"
    "v_lshlrev_b32 v100, 16, v166\n"
    "v_and_b32 v101, 0xffff0000, v166\n"
    "v_mfma_f32_16x16x32_bf16 v[244:247], v[68:71], v[28:31], v[244:247]\n"
    "v_lshlrev_b32 v102, 16, v167\n"
    "v_and_b32 v103, 0xffff0000, v167\n"
    "v_mfma_f32_16x16x32_bf16 v[240:243], v[72:75], v[32:35], v[240:243]\n"
    "v_lshlrev_b32 v104, 16, v170\n"
    "v_and_b32 v105, 0xffff0000, v170\n"
    "v_mfma_f32_16x16x32_bf16 v[244:247], v[72:75], v[36:39], v[244:247]\n"
    "v_lshlrev_b32 v106, 16, v171\n"
    "v_and_b32 v107, 0xffff0000, v171\n"
    "v_mfma_f32_16x16x32_bf16 v[240:243], v[76:79], v[40:43], v[240:243]\n"
    "v_pk_add_f32 v[0:1], v[232:233], v[92:93]\n"
    "v_pk_add_f32 v[2:3], v[234:235], v[94:95]\n"
    "v_mfma_f32_16x16x32_bf16 v[244:247], v[76:79], v[44:47], v[244:247]\n"
    "v_pk_add_f32 v[4:5], v[236:237], v[96:97]\n"
    "v_pk_add_f32 v[6:7], v[238:239], v[98:99]\n"
    "s_nop 1\n"
    "v_pk_add_f32 v[8:9], v[240:241], v[100:101]\n"
    "v_pk_add_f32 v[10:11], v[242:243], v[102:103]\n"
    "s_nop 1\n"
    "v_pk_add_f32 v[12:13], v[244:245], v[104:105]\n"
    "v_pk_add_f32 v[14:15], v[246:247], v[106:107]\n"
    ".Lrc2_%=:\n"
    "s_sleep 8\n"
    "buffer_load_dwordx4 v[128:131], v172, s[52:55], s68 offen offset:0 sc1\n"
    "buffer_load_dwordx4 v[132:135], v172, s[52:55], s68 offen offset:1024 sc1\n"
    "buffer_load_dword v136, v176, s[52:55], s68 offen offset:2048 sc1\n"
    "s_sleep 2\n"
    "buffer_load_dwordx4 v[138:141], v172, s[52:55], s69 offen offset:0 sc1\n"
    "buffer_load_dwordx4 v[142:145], v172, s[52:55], s69 offen offset:1024 sc1\n"
    "buffer_load_dword v146, v176, s[52:55], s69 offen offset:2048 sc1\n"
    "s_sleep 2\n"
    "buffer_load_dwordx4 v[148:151], v172, s[52:55], s70 offen offset:0 sc1\n"
    "buffer_load_dwordx4 v[152:155], v172, s[52:55], s70 offen offset:1024 sc1\n"
    "buffer_load_dword v156, v176, s[52:55], s70 offen offset:2048 sc1\n"
    "s_waitcnt vmcnt(6)\n"
    "v_perm_b32 v64, v135, v131, s75\n"
    "v_xor_b32 v64, v64, v136\n"
    "v_cmp_ne_u32 vcc, v64, v187\n"
    "s_cbranch_vccnz .Lcfa01_%=\n"
    "v_lshlrev_b32 v16, 16, v128\n"
    "v_and_b32 v17, 0xffff0000, v128\n"
    "v_lshlrev_b32 v18, 16, v129\n"
    "v_and_b32 v19, 0xffff0000, v129\n"
    "v_lshlrev_b32 v20, 16, v130\n"
    "v_and_b32 v21, 0xffff0000, v130\n"
    "v_lshlrev_b32 v22, 16, v131\n"
    "v_lshlrev_b32 v23, 16, v136\n"
    "v_lshlrev_b32 v24, 16, v132\n"
    "v_and_b32 v25, 0xffff0000, v132\n"
    "v_lshlrev_b32 v26, 16, v133\n"
    "v_and_b32 v27, 0xffff0000, v133\n"
    "v_lshlrev_b32 v28, 16, v134\n"
    "v_and_b32 v29, 0xffff0000, v134\n"
    "v_lshlrev_b32 v30, 16, v135\n"
    "v_and_b32 v31, 0xffff0000, v136\n"
    "s_bitset0_b32 s76, 0\n"
    ".Lcfa01_%=:\n"
    "s_waitcnt vmcnt(3)\n"
    "v_perm_b32 v64, v145, v141, s75\n"
    "v_xor_b32 v64, v64, v146\n"
    "v_cmp_ne_u32 vcc, v64, v187\n"
    "s_cbranch_vccnz .Lcfa11_%=\n"
    "v_lshlrev_b32 v32, 16, v138\n"
    "v_and_b32 v33, 0xffff0000, v138\n"
    "v_lshlrev_b32 v34, 16, v139\n"
    "v_and_b32 v35, 0xffff0000, v139\n"
    "v_lshlrev_b32 v36, 16, v140\n"
    "v_and_b32 v37, 0xffff0000, v140\n"
    "v_lshlrev_b32 v38, 16, v141\n"
    "v_lshlrev_b32 v39, 16, v146\n"
    "v_lshlrev_b32 v40, 16, v142\n"
    "v_and_b32 v41, 0xffff0000, v142\n"
    "v_lshlrev_b32 v42, 16, v143\n"
    "v_and_b32 v43, 0xffff0000, v143\n"
    "v_lshlrev_b32 v44, 16, v144\n"
    "v_and_b32 v45, 0xffff0000, v144\n"
    "v_lshlrev_b32 v46, 16, v145\n"
    "v_and_b32 v47, 0xffff0000, v146\n"
    "s_bitset0_b32 s76, 1\n"
    ".Lcfa11_%=:\n"
    "s_waitcnt vmcnt(0)\n"
    "v_perm_b32 v64, v155, v151, s75\n"
    "v_xor_b32 v64, v64, v156\n"
    "v_cmp_ne_u32 vcc, v64, v187\n"
    "s_cbranch_vccnz .Lcfa21_%=\n"
    "v_lshlrev_b32 v48, 16, v148\n"
    "v_and_b32 v49, 0xffff0000, v148\n"
    "v_lshlrev_b32 v50, 16, v149\n"
    "v_and_b32 v51, 0xffff0000, v149\n"
    "v_lshlrev_b32 v52, 16, v150\n"
    "v_and_b32 v53, 0xffff0000, v150\n"
    "v_lshlrev_b32 v54, 16, v151\n"
    "v_lshlrev_b32 v55, 16, v156\n"
    "v_lshlrev_b32 v56, 16, v152\n"
    "v_and_b32 v57, 0xffff0000, v152\n"
    "v_lshlrev_b32 v58, 16, v153\n"
    "v_and_b32 v59, 0xffff0000, v153\n"
    "v_lshlrev_b32 v60, 16, v154\n"
    "v_and_b32 v61, 0xffff0000, v154\n"
    "v_lshlrev_b32 v62, 16, v155\n"
    "v_and_b32 v63, 0xffff0000, v156\n"
    "s_bitset0_b32 s76, 2\n"
    ".Lcfa21_%=:\n"
    "s_cmp_eq_u32 s76, 0\n"
    "s_cbranch_scc1 .Lcd1_%=\n"
    "s_bitcmp1_b32 s76, 0\n"
    "s_cbranch_scc0 .Lcsi10_%=\n"
    "buffer_load_dwordx4 v[128:131], v172, s[52:55], s68 offen offset:0 sc1\n"
    "buffer_load_dwordx4 v[132:135], v172, s[52:55], s68 offen offset:1024 sc1\n"
    "buffer_load_dword v136, v176, s[52:55], s68 offen offset:2048 sc1\n"
    ".Lcsi10_%=:\n"
    "s_bitcmp1_b32 s76, 1\n"
    "s_cbranch_scc0 .Lcsi11_%=\n"
    "buffer_load_dwordx4 v[138:141], v172, s[52:55], s69 offen offset:0 sc1\n"
    "buffer_load_dwordx4 v[142:145], v172, s[52:55], s69 offen offset:1024 sc1\n"
    "buffer_load_dword v146, v176, s[52:55], s69 offen offset:2048 sc1\n"
    ".Lcsi11_%=:\n"
    "s_bitcmp1_b32 s76, 2\n"
    "s_cbranch_scc0 .Lcsi12_%=\n"
    "buffer_load_dwordx4 v[148:151], v172, s[52:55], s70 offen offset:0 sc1\n"
    "buffer_load_dwordx4 v[152:155], v172, s[52:55], s70 offen offset:1024 sc1\n"
    "buffer_load_dword v156, v176, s[52:55], s70 offen offset:2048 sc1\n"
    ".Lcsi12_%=:\n"
    ".Lcl1x_%=:\n"
    "s_waitcnt vmcnt(0)\n"
    "s_bitcmp1_b32 s76, 0\n"
    "s_cbranch_scc0 .Lcsl010_%=\n"
    "v_perm_b32 v64, v135, v131, s75\n"
    "v_xor_b32 v64, v64, v136\n"
    "v_cmp_ne_u32 vcc, v64, v187\n"
    "s_cbranch_vccnz .Lcfb001_%=\n"
    "v_lshlrev_b32 v16, 16, v128\n"
    "v_and_b32 v17, 0xffff0000, v128\n"
    "v_lshlrev_b32 v18, 16, v129\n"
    "v_and_b32 v19, 0xffff0000, v129\n"
    "v_lshlrev_b32 v20, 16, v130\n"
    "v_and_b32 v21, 0xffff0000, v130\n"
    "v_lshlrev_b32 v22, 16, v131\n"
    "v_lshlrev_b32 v23, 16, v136\n"
    "v_lshlrev_b32 v24, 16, v132\n"
    "v_and_b32 v25, 0xffff0000, v132\n"
    "v_lshlrev_b32 v26, 16, v133\n"
    "v_and_b32 v27, 0xffff0000, v133\n"
    "v_lshlrev_b32 v28, 16, v134\n"
    "v_and_b32 v29, 0xffff0000, v134\n"
    "v_lshlrev_b32 v30, 16, v135\n"
    "v_and_b32 v31, 0xffff0000, v136\n"
    "s_bitset0_b32 s76, 0\n"
    "s_branch .Lcnb001_%=\n"
    ".Lcfb001_%=:\n"
    "buffer_load_dwordx4 v[128:131], v172, s[52:55], s68 offen offset:0 sc1\n"
    "buffer_load_dwordx4 v[132:135], v172, s[52:55], s68 offen offset:1024 sc1\n"
    "buffer_load_dword v136, v176, s[52:55], s68 offen offset:2048 sc1\n"
    ".Lcnb001_%=:\n"
    ".Lcsl010_%=:\n"
    "s_bitcmp1_b32 s76, 1\n"
    "s_cbranch_scc0 .Lcsl011_%=\n"
    "v_perm_b32 v64, v145, v141, s75\n"
    "v_xor_b32 v64, v64, v146\n"
    "v_cmp_ne_u32 vcc, v64, v187\n"
    "s_cbranch_vccnz .Lcfb011_%=\n"
    "v_lshlrev_b32 v32, 16, v138\n"
    "v_and_b32 v33, 0xffff0000, v138\n"
    "v_lshlrev_b32 v34, 16, v139\n"
    "v_and_b32 v35, 0xffff0000, v139\n"
    "v_lshlrev_b32 v36, 16, v140\n"
    "v_and_b32 v37, 0xffff0000, v140\n"
    "v_lshlrev_b32 v38, 16, v141\n"
    "v_lshlrev_b32 v39, 16, v146\n"
    "v_lshlrev_b32 v40, 16, v142\n"
    "v_and_b32 v41, 0xffff0000, v142\n"
    "v_lshlrev_b32 v42, 16, v143\n"
    "v_and_b32 v43, 0xffff0000, v143\n"
    "v_lshlrev_b32 v44, 16, v144\n"
    "v_and_b32 v45, 0xffff0000, v144\n"
    "v_lshlrev_b32 v46, 16, v145\n"
    "v_and_b32 v47, 0xffff0000, v146\n"
    "s_bitset0_b32 s76, 1\n"
    "s_branch .Lcnb011_%=\n"
    ".Lcfb011_%=:\n"
    "buffer_load_dwordx4 v[138:141], v172, s[52:55], s69 offen offset:0 sc1\n"
    "buffer_load_dwordx4 v[142:145], v172, s[52:55], s69 offen offset:1024 sc1\n"
    "buffer_load_dword v146, v176, s[52:55], s69 offen offset:2048 sc1\n"
    ".Lcnb011_%=:\n"
    ".Lcsl011_%=:\n"
    "s_bitcmp1_b32 s76, 2\n"
    "s_cbranch_scc0 .Lcsl012_%=\n"
    "v_perm_b32 v64, v155, v151, s75\n"
    "v_xor_b32 v64, v64, v156\n"
    "v_cmp_ne_u32 vcc, v64, v187\n"
    "s_cbranch_vccnz .Lcfb021_%=\n"
    "v_lshlrev_b32 v48, 16, v148\n"
    "v_and_b32 v49, 0xffff0000, v148\n"
    "v_lshlrev_b32 v50, 16, v149\n"
    "v_and_b32 v51, 0xffff0000, v149\n"
    "v_lshlrev_b32 v52, 16, v150\n"
    "v_and_b32 v53, 0xffff0000, v150\n"
    "v_lshlrev_b32 v54, 16, v151\n"
    "v_lshlrev_b32 v55, 16, v156\n"
    "v_lshlrev_b32 v56, 16, v152\n"
    "v_and_b32 v57, 0xffff0000, v152\n"
    "v_lshlrev_b32 v58, 16, v153\n"
    "v_and_b32 v59, 0xffff0000, v153\n"
    "v_lshlrev_b32 v60, 16, v154\n"
    "v_and_b32 v61, 0xffff0000, v154\n"
    "v_lshlrev_b32 v62, 16, v155\n"
    "v_and_b32 v63, 0xffff0000, v156\n"
    "s_bitset0_b32 s76, 2\n"
    "s_branch .Lcnb021_%=\n"
    ".Lcfb021_%=:\n"
    "buffer_load_dwordx4 v[148:151], v172, s[52:55], s70 offen offset:0 sc1\n"
    "buffer_load_dwordx4 v[152:155], v172, s[52:55], s70 offen offset:1024 sc1\n"
    "buffer_load_dword v156, v176, s[52:55], s70 offen offset:2048 sc1\n"
    ".Lcnb021_%=:\n"
    ".Lcsl012_%=:\n"
    "s_cmp_eq_u32 s76, 0\n"
    "s_cbranch_scc1 .Lcd1_%=\n"
    "s_sub_u32 s83, s83, 1\n"
    "s_cbranch_scc0 .Lcl1x_%=\n"
    ".Lcd1_%=:\n"
    "v_pk_add_f32 v[0:1], v[0:1], v[16:17]\n"
    "v_pk_add_f32 v[2:3], v[2:3], v[18:19]\n"
    "v_pk_add_f32 v[4:5], v[4:5], v[20:21]\n"
    "v_pk_add_f32 v[6:7], v[6:7], v[22:23]\n"
    "v_pk_add_f32 v[8:9], v[8:9], v[24:25]\n"
    "v_pk_add_f32 v[10:11], v[10:11], v[26:27]\n"
    "v_pk_add_f32 v[12:13], v[12:13], v[28:29]\n"
    "v_pk_add_f32 v[14:15], v[14:15], v[30:31]\n"
    "v_pk_add_f32 v[0:1], v[0:1], v[32:33]\n"
    "v_pk_add_f32 v[2:3], v[2:3], v[34:35]\n"
    "v_pk_add_f32 v[4:5], v[4:5], v[36:37]\n"
    "v_pk_add_f32 v[6:7], v[6:7], v[38:39]\n"
    "v_pk_add_f32 v[8:9], v[8:9], v[40:41]\n"
    "v_pk_add_f32 v[10:11], v[10:11], v[42:43]\n"
    "v_pk_add_f32 v[12:13], v[12:13], v[44:45]\n"
    "v_pk_add_f32 v[14:15], v[14:15], v[46:47]\n"
    "v_pk_add_f32 v[0:1], v[0:1], v[48:49]\n"
    "v_pk_add_f32 v[2:3], v[2:3], v[50:51]\n"
    "v_pk_add_f32 v[4:5], v[4:5], v[52:53]\n"
    "v_pk_add_f32 v[6:7], v[6:7], v[54:55]\n"
    "v_pk_add_f32 v[8:9], v[8:9], v[56:57]\n"
    "v_pk_add_f32 v[10:11], v[10:11], v[58:59]\n"
    "v_pk_add_f32 v[12:13], v[12:13], v[60:61]\n"
    "v_pk_add_f32 v[14:15], v[14:15], v[62:63]\n"
    "v_cvt_pk_bf16_f32 v72, v0, v1\n"
    "v_cvt_pk_bf16_f32 v73, v2, v3\n"
    "v_cvt_pk_bf16_f32 v74, v8, v9\n"
    "v_cvt_pk_bf16_f32 v75, v10, v11\n"
    "buffer_store_dwordx4 v[72:75], v199, s[40:43], 0 offen offset:0\n"
    "v_cvt_pk_bf16_f32 v76, v4, v5\n"
    "v_cvt_pk_bf16_f32 v77, v6, v7\n"
    "v_cvt_pk_bf16_f32 v78, v12, v13\n"
    "v_cvt_pk_bf16_f32 v79, v14, v15\n"
    "buffer_store_dwordx4 v[76:79], v199, s[40:43], 0 offen offset:3584\n"
    "s_endpgm\n"
    ".Lrset_%=:\n"
    "s_waitcnt vmcnt(0)\n"
    "s_barrier\n"
    "s_endpgm\n"
    ".Lhlp_%=:\n"
    "s_setprio 1\n"
    "v_mov_b32 v6, 0\n"
    "s_lshl2_add_u32 s62, s24, s26\n"
    "s_lshl4_add_u32 s67, s27, s62\n"
    "s_lshl_b32 s67, s67, 7\n"
    "s_lshl4_add_u32 s62, s62, s27\n"
    "s_lshl_b32 s62, s62, 7\n"
    "v_lshlrev_b32 v10, 4, v0\n"
    "v_mov_b32 v12, -1\n"
    "v_mov_b32 v13, -1\n"
    "v_mov_b32 v14, -1\n"
    "v_mov_b32 v15, -1\n"
    "s_mul_i32 s65, s24, 7\n"
    "v_and_b32 v20, 7, v0\n"
    "v_min_u32 v20, 6, v20\n"
    "v_add_u32 v20, s65, v20\n"
    "v_and_b32 v21, 1, v20\n"
    "v_lshlrev_b32 v21, 7, v21\n"
    "v_lshrrev_b32 v20, 1, v20\n"
    "v_add_u32 v20, v20, v21\n"
    "v_lshlrev_b32 v20, 7, v20\n"
    "v_add_u32 v7, 669696, v20\n"
    "s_lshr_b32 s65, s65, 1\n"
    "v_and_b32 v20, 3, v0\n"
    "v_add_u32 v21, s65, v20\n"
    "v_cmp_lt_u32 vcc, 6, v21\n"
    "s_nop 1\n"
    "v_addc_co_u32 v21, vcc, 0, v21, vcc\n"
    "v_lshl_add_u32 v20, v20, 2, s26\n"
    "v_cmp_gt_u32 vcc, 4, v0\n"
    "s_nop 1\n"
    "v_cndmask_b32 v20, v20, v21, vcc\n"
    "s_lshl_b32 s66, s27, 11\n"
    "s_add_u32 s66, s66, 786432\n"
    "v_lshl_add_u32 v9, v20, 7, s66\n"
    "s_mul_i32 s65, s26, 7\n"
    "s_add_u32 s66, s65, 6\n"
    "s_lshr_b32 s65, s65, 2\n"
    "s_lshr_b32 s66, s66, 2\n"
    "v_lshrrev_b32 v20, 3, v0\n"
    "v_add_u32 v20, s65, v20\n"
    "v_min_u32 v20, s66, v20\n"
    "v_and_b32 v21, 7, v0\n"
    "v_lshl_add_u32 v20, v20, 4, v21\n"
    "s_lshr_b32 s65, s24, 1\n"
    "s_and_b32 s66, s24, 1\n"
    "s_lshl4_add_u32 s65, s65, s66\n"
    "s_lshl_b32 s65, s65, 10\n"
    "s_add_u32 s65, s65, 655360\n"
    "v_lshl_add_u32 v8, v20, 7, s65\n"
    "s_cmp_eq_u32 s29, 1\n"
    "s_cbranch_scc1 .Lhidle_%=\n"
    "s_barrier\n"
    "s_waitcnt lgkmcnt(0)\n"
    "s_mov_b32 s52, s20\n"
    "s_and_b32 s53, s21, 0xffff\n"
    "s_mov_b32 s54, 25165824\n"
    "s_mov_b32 s55, 0x20000\n"
    "s_mov_b64 exec, 0x1\n"
    "buffer_load_dwordx2 v[4:5], v6, s[52:55], 0 offen sc1\n"
    "s_mov_b64 exec, -1\n"
    "s_barrier\n"
    "s_barrier\n"
    "s_barrier\n"
    "s_barrier\n"
    "s_waitcnt vmcnt(0)\n"
    "v_readfirstlane_b32 s65, v4\n"
    "v_readfirstlane_b32 s66, v5\n"
    "s_mul_i32 s60, s65, 0x4c957f2d\n"
    "s_mul_hi_u32 s61, s65, 0x4c957f2d\n"
    "s_mul_i32 s65, s65, 0x5851f42d\n"
    "s_mul_i32 s66, s66, 0x4c957f2d\n"
    "s_add_u32 s61, s61, s65\n"
    "s_add_u32 s61, s61, s66\n"
    "s_add_u32 s60, s60, 0xf767814f\n"
    "s_addc_u32 s61, s61, 0x14057b7e\n"
    "v_mov_b32 v4, s60\n"
    "v_mov_b32 v5, s61\n"
    "v_mov_b32 v16, 154632\n"
    "ds_write_b64 v16, v[4:5]\n"
    "s_waitcnt lgkmcnt(0)\n"
    "s_barrier\n"
    "s_barrier\n"
    "s_barrier\n"
    "s_barrier\n"
    "s_mov_b64 exec, 0x7f\n"
    "buffer_load_dwordx2 v[24:25], v7, s[52:55], 0 offen sc1\n"
    "s_mov_b64 exec, -1\n"
    "s_barrier\n"
    "s_add_u32 s65, s62, 655360\n"
    "s_mov_b64 exec, 1\n"
    "buffer_store_dwordx2 v[4:5], v6, s[52:55], s65 offen sc1\n"
    "s_mov_b64 exec, -1\n"
    "s_mov_b64 exec, 0x7f\n"
    "buffer_load_dwordx2 v[26:27], v7, s[52:55], 0 offen sc1\n"
    "s_mov_b64 exec, -1\n"
    "s_barrier\n"
    "s_mov_b64 exec, 0x7f\n"
    "s_waitcnt vmcnt(2)\n"
    "v_cmp_ne_u64 vcc, v[24:25], v[4:5]\n"
    "s_cbranch_vccz .Lhq1_%=\n"
    "s_waitcnt vmcnt(0)\n"
    "v_cmp_ne_u64 vcc, v[26:27], v[4:5]\n"
    "s_cbranch_vccz .Lhq1_%=\n"
    "buffer_load_dwordx2 v[24:25], v7, s[52:55], 0 offen sc1\n"
    "s_sleep 4\n"
    "buffer_load_dwordx2 v[26:27], v7, s[52:55], 0 offen sc1\n"
    "s_sleep 4\n"
    "buffer_load_dwordx2 v[28:29], v7, s[52:55], 0 offen sc1\n"
    "s_sleep 4\n"
    "s_mov_b64 exec, 0x7f\n"
    "s_mov_b32 s64, 16384\n"
    ".Lhp1_%=:\n"
    "s_waitcnt vmcnt(2)\n"
    "v_cmp_ne_u64 vcc, v[24:25], v[4:5]\n"
    "s_cbranch_vccz .Lhq1_%=\n"
    "buffer_load_dwordx2 v[24:25], v7, s[52:55], 0 offen sc1\n"
    "s_sleep 4\n"
    "s_waitcnt vmcnt(2)\n"
    "v_cmp_ne_u64 vcc, v[26:27], v[4:5]\n"
    "s_cbranch_vccz .Lhq1_%=\n"
    "buffer_load_dwordx2 v[26:27], v7, s[52:55], 0 offen sc1\n"
    "s_sleep 4\n"
    "s_waitcnt vmcnt(2)\n"
    "v_cmp_ne_u64 vcc, v[28:29], v[4:5]\n"
    "s_cbranch_vccz .Lhq1_%=\n"
    "buffer_load_dwordx2 v[28:29], v7, s[52:55], 0 offen sc1\n"
    "s_sleep 4\n"
    "s_sub_u32 s64, s64, 1\n"
    "s_cbranch_scc0 .Lhp1_%=\n"
    ".Lhq1_%=:\n"
    "s_mov_b64 exec, -1\n"
    "s_barrier\n"
    "s_mov_b64 exec, 0xffffff\n"
    "buffer_load_dwordx2 v[32:33], v8, s[52:55], 0 offen sc1\n"
    "s_mov_b64 exec, -1\n"
    "s_barrier\n"
    "s_mov_b64 exec, 0xffffff\n"
    "buffer_load_dwordx2 v[34:35], v8, s[52:55], 0 offen sc1\n"
    "s_mov_b64 exec, -1\n"
    "s_barrier\n"
    "s_mov_b64 exec, 0xffffff\n"
    "buffer_load_dwordx2 v[36:37], v8, s[52:55], 0 offen sc1\n"
    "s_mov_b64 exec, -1\n"
    "s_mov_b64 exec, 0xffffff\n"
    "s_waitcnt vmcnt(2)\n"
    "v_cmp_ne_u64 vcc, v[32:33], v[4:5]\n"
    "s_cbranch_vccz .Lhq2_%=\n"
    "s_waitcnt vmcnt(1)\n"
    "v_cmp_ne_u64 vcc, v[34:35], v[4:5]\n"
    "s_cbranch_vccz .Lhq2_%=\n"
    "s_waitcnt vmcnt(0)\n"
    "v_cmp_ne_u64 vcc, v[36:37], v[4:5]\n"
    "s_cbranch_vccz .Lhq2_%=\n"
    "buffer_load_dwordx2 v[32:33], v8, s[52:55], 0 offen sc1\n"
    "s_sleep 4\n"
    "buffer_load_dwordx2 v[34:35], v8, s[52:55], 0 offen sc1\n"
    "s_sleep 4\n"
    "buffer_load_dwordx2 v[36:37], v8, s[52:55], 0 offen sc1\n"
    "s_sleep 4\n"
    "s_mov_b64 exec, 0xffffff\n"
    "s_mov_b32 s64, 16384\n"
    ".Lhp2_%=:\n"
    "s_waitcnt vmcnt(2)\n"
    "v_cmp_ne_u64 vcc, v[32:33], v[4:5]\n"
    "s_cbranch_vccz .Lhq2_%=\n"
    "buffer_load_dwordx2 v[32:33], v8, s[52:55], 0 offen sc1\n"
    "s_sleep 4\n"
    "s_waitcnt vmcnt(2)\n"
    "v_cmp_ne_u64 vcc, v[34:35], v[4:5]\n"
    "s_cbranch_vccz .Lhq2_%=\n"
    "buffer_load_dwordx2 v[34:35], v8, s[52:55], 0 offen sc1\n"
    "s_sleep 4\n"
    "s_waitcnt vmcnt(2)\n"
    "v_cmp_ne_u64 vcc, v[36:37], v[4:5]\n"
    "s_cbranch_vccz .Lhq2_%=\n"
    "buffer_load_dwordx2 v[36:37], v8, s[52:55], 0 offen sc1\n"
    "s_sleep 4\n"
    "s_sub_u32 s64, s64, 1\n"
    "s_cbranch_scc0 .Lhp2_%=\n"
    ".Lhq2_%=:\n"
    "s_mov_b64 exec, -1\n"
    "s_barrier\n"
    ".Lhjoin_%=:\n"
    "s_barrier\n"
    "s_barrier\n"
    "s_barrier\n"
    "s_barrier\n"
    "s_barrier\n"
    "s_barrier\n"
    "s_barrier\n"
    "s_waitcnt vmcnt(0)\n"
    "s_barrier\n"
    "s_add_u32 s65, s67, 786432\n"
    "s_mov_b64 exec, 1\n"
    "buffer_store_dwordx2 v[4:5], v6, s[52:55], s65 offen sc1\n"
    "s_mov_b64 exec, -1\n"
    "s_mov_b32 s63, 1\n"
    ".Lhd_%=:\n"
    "s_barrier\n"
    "s_sub_u32 s63, s63, 1\n"
    "s_cbranch_scc0 .Lhd_%=\n"
    "s_barrier\n"
    "s_mov_b64 exec, 0xf\n"
    "buffer_load_dwordx2 v[40:41], v9, s[52:55], 0 offen sc1\n"
    "s_mov_b64 exec, -1\n"
    "s_barrier\n"
    "s_mov_b64 exec, 0xf\n"
    "buffer_load_dwordx2 v[42:43], v9, s[52:55], 0 offen sc1\n"
    "s_mov_b64 exec, -1\n"
    "s_barrier\n"
    "s_mov_b64 exec, 0xf\n"
    "buffer_load_dwordx2 v[44:45], v9, s[52:55], 0 offen sc1\n"
    "s_mov_b64 exec, -1\n"
    "s_mov_b64 exec, 0xf\n"
    "s_mov_b32 s64, 16384\n"
    ".Lhp3_%=:\n"
    "s_waitcnt vmcnt(2)\n"
    "v_cmp_ne_u64 vcc, v[40:41], v[4:5]\n"
    "s_cbranch_vccz .Lhq3_%=\n"
    "buffer_load_dwordx2 v[40:41], v9, s[52:55], 0 offen sc1\n"
    "s_sleep 4\n"
    "s_waitcnt vmcnt(2)\n"
    "v_cmp_ne_u64 vcc, v[42:43], v[4:5]\n"
    "s_cbranch_vccz .Lhq3_%=\n"
    "buffer_load_dwordx2 v[42:43], v9, s[52:55], 0 offen sc1\n"
    "s_sleep 4\n"
    "s_waitcnt vmcnt(2)\n"
    "v_cmp_ne_u64 vcc, v[44:45], v[4:5]\n"
    "s_cbranch_vccz .Lhq3_%=\n"
    "buffer_load_dwordx2 v[44:45], v9, s[52:55], 0 offen sc1\n"
    "s_sleep 4\n"
    "s_sub_u32 s64, s64, 1\n"
    "s_cbranch_scc0 .Lhp3_%=\n"
    ".Lhq3_%=:\n"
    "s_mov_b64 exec, -1\n"
    "s_sub_u32 s63, 7, s30\n"
    ".Lhe_%=:\n"
    "s_barrier\n"
    "s_sub_u32 s63, s63, 1\n"
    "s_cbranch_scc0 .Lhe_%=\n"
    "s_branch .Lrset_%=\n"
    ".Lhidle_%=:\n"
    "s_waitcnt lgkmcnt(0)\n"
    "s_mov_b32 s52, s20\n"
    "s_and_b32 s53, s21, 0xffff\n"
    "s_mov_b32 s54, 25165824\n"
    "s_mov_b32 s55, 0x20000\n"
    "s_mov_b64 exec, 0x1\n"
    "buffer_load_dwordx2 v[4:5], v6, s[52:55], 0 offen sc1\n"
    "s_mov_b64 exec, -1\n"
    "s_waitcnt vmcnt(0)\n"
    "v_readfirstlane_b32 s65, v4\n"
    "v_readfirstlane_b32 s66, v5\n"
    "s_mul_i32 s60, s65, 0x4c957f2d\n"
    "s_mul_hi_u32 s61, s65, 0x4c957f2d\n"
    "s_mul_i32 s65, s65, 0x5851f42d\n"
    "s_mul_i32 s66, s66, 0x4c957f2d\n"
    "s_add_u32 s61, s61, s65\n"
    "s_add_u32 s61, s61, s66\n"
    "s_add_u32 s60, s60, 0xf767814f\n"
    "s_addc_u32 s61, s61, 0x14057b7e\n"
    "v_mov_b32 v4, s60\n"
    "v_mov_b32 v5, s61\n"
    "v_mov_b32 v16, 154632\n"
    "ds_write_b64 v16, v[4:5]\n"
    "s_waitcnt lgkmcnt(0)\n"
    "s_barrier\n"
    "s_barrier\n"
    "s_add_u32 s65, s62, 655360\n"
    "s_mov_b64 exec, 1\n"
    "buffer_store_dwordx2 v[4:5], v6, s[52:55], s65 offen sc1\n"
    "s_mov_b64 exec, -1\n"
    "s_mov_b64 exec, 0x7f\n"
    "buffer_load_dwordx2 v[24:25], v7, s[52:55], 0 offen sc1\n"
    "s_mov_b64 exec, -1\n"
    "s_mov_b64 exec, 0x7f\n"
    "s_mov_b32 s64, 16384\n"
    ".Lhp4_%=:\n"
    "s_waitcnt vmcnt(0)\n"
    "v_cmp_ne_u64 vcc, v[24:25], v[4:5]\n"
    "s_cbranch_vccz .Lhq4_%=\n"
    "buffer_load_dwordx2 v[24:25], v7, s[52:55], 0 offen sc1\n"
    "s_sleep 4\n"
    "s_sub_u32 s64, s64, 1\n"
    "s_cbranch_scc0 .Lhp4_%=\n"
    ".Lhq4_%=:\n"
    "s_mov_b64 exec, -1\n"
    "s_mov_b64 exec, 0xffffff\n"
    "buffer_load_dwordx2 v[32:33], v8, s[52:55], 0 offen sc1\n"
    "s_mov_b64 exec, -1\n"
    "s_mov_b64 exec, 0xffffff\n"
    "buffer_load_dwordx2 v[34:35], v8, s[52:55], 0 offen sc1\n"
    "s_mov_b64 exec, -1\n"
    "s_mov_b64 exec, 0xffffff\n"
    "buffer_load_dwordx2 v[36:37], v8, s[52:55], 0 offen sc1\n"
    "s_mov_b64 exec, -1\n"
    "s_mov_b64 exec, 0xffffff\n"
    "s_mov_b32 s64, 16384\n"
    ".Lhp5_%=:\n"
    "s_waitcnt vmcnt(2)\n"
    "v_cmp_ne_u64 vcc, v[32:33], v[4:5]\n"
    "s_cbranch_vccz .Lhq5_%=\n"
    "buffer_load_dwordx2 v[32:33], v8, s[52:55], 0 offen sc1\n"
    "s_sleep 4\n"
    "s_waitcnt vmcnt(2)\n"
    "v_cmp_ne_u64 vcc, v[34:35], v[4:5]\n"
    "s_cbranch_vccz .Lhq5_%=\n"
    "buffer_load_dwordx2 v[34:35], v8, s[52:55], 0 offen sc1\n"
    "s_sleep 4\n"
    "s_waitcnt vmcnt(2)\n"
    "v_cmp_ne_u64 vcc, v[36:37], v[4:5]\n"
    "s_cbranch_vccz .Lhq5_%=\n"
    "buffer_load_dwordx2 v[36:37], v8, s[52:55], 0 offen sc1\n"
    "s_sleep 4\n"
    "s_sub_u32 s64, s64, 1\n"
    "s_cbranch_scc0 .Lhp5_%=\n"
    ".Lhq5_%=:\n"
    "s_mov_b64 exec, -1\n"
    "s_barrier\n"
    "s_cmp_eq_u32 s2, 26\n"
    "s_cbranch_scc0 .Lhjoin_%=\n"
    "v_lshlrev_b32 v20, 7, v0\n"
    "v_add_u32 v20, 655360, v20\n"
    "s_waitcnt vmcnt(0)\n"
    "v_add_u32 v21, 0, v20\n"
    "s_mov_b64 exec, -1\n"
    "buffer_load_dwordx2 v[48:49], v21, s[52:55], 0 offen sc1\n"
    "s_mov_b64 exec, -1\n"
    "v_add_u32 v22, 8192, v20\n"
    "s_mov_b64 exec, -1\n"
    "buffer_load_dwordx2 v[50:51], v22, s[52:55], 0 offen sc1\n"
    "s_mov_b64 exec, -1\n"
    "v_add_u32 v23, 16384, v20\n"
    "s_mov_b64 exec, -1\n"
    "buffer_load_dwordx2 v[52:53], v23, s[52:55], 0 offen sc1\n"
    "s_mov_b64 exec, -1\n"
    "v_add_u32 v24, 24576, v20\n"
    "s_mov_b64 exec, -1\n"
    "buffer_load_dwordx2 v[54:55], v24, s[52:55], 0 offen sc1\n"
    "s_mov_b64 exec, -1\n"
    "s_mov_b32 s64, 16384\n"
    ".Lhp6_%=:\n"
    "s_waitcnt vmcnt(0)\n"
    "v_cmp_ne_u64 vcc, v[48:49], v[4:5]\n"
    "s_cbranch_vccz .Lhq6_%=\n"
    "buffer_load_dwordx2 v[48:49], v21, s[52:55], 0 offen sc1\n"
    "s_sleep 4\n"
    "s_sub_u32 s64, s64, 1\n"
    "s_cbranch_scc0 .Lhp6_%=\n"
    ".Lhq6_%=:\n"
    "s_mov_b64 exec, -1\n"
    "s_mov_b32 s64, 16384\n"
    ".Lhp7_%=:\n"
    "s_waitcnt vmcnt(0)\n"
    "v_cmp_ne_u64 vcc, v[50:51], v[4:5]\n"
    "s_cbranch_vccz .Lhq7_%=\n"
    "buffer_load_dwordx2 v[50:51], v22, s[52:55], 0 offen sc1\n"
    "s_sleep 4\n"
    "s_sub_u32 s64, s64, 1\n"
    "s_cbranch_scc0 .Lhp7_%=\n"
    ".Lhq7_%=:\n"
    "s_mov_b64 exec, -1\n"
    "s_mov_b32 s64, 16384\n"
    ".Lhp8_%=:\n"
    "s_waitcnt vmcnt(0)\n"
    "v_cmp_ne_u64 vcc, v[52:53], v[4:5]\n"
    "s_cbranch_vccz .Lhq8_%=\n"
    "buffer_load_dwordx2 v[52:53], v23, s[52:55], 0 offen sc1\n"
    "s_sleep 4\n"
    "s_sub_u32 s64, s64, 1\n"
    "s_cbranch_scc0 .Lhp8_%=\n"
    ".Lhq8_%=:\n"
    "s_mov_b64 exec, -1\n"
    "s_mov_b32 s64, 16384\n"
    ".Lhp9_%=:\n"
    "s_waitcnt vmcnt(0)\n"
    "v_cmp_ne_u64 vcc, v[54:55], v[4:5]\n"
    "s_cbranch_vccz .Lhq9_%=\n"
    "buffer_load_dwordx2 v[54:55], v24, s[52:55], 0 offen sc1\n"
    "s_sleep 4\n"
    "s_sub_u32 s64, s64, 1\n"
    "s_cbranch_scc0 .Lhp9_%=\n"
    ".Lhq9_%=:\n"
    "s_mov_b64 exec, -1\n"
    "s_add_u32 s65, 0, 0\n"
    "s_mov_b64 exec, 1\n"
    "buffer_store_dwordx2 v[4:5], v6, s[52:55], s65 offen sc1\n"
    "s_mov_b64 exec, -1\n"
    "s_branch .Lhjoin_%=\n"
    ".Lkend_%=:\n"
    :
    : "{s[0:1]}"(__builtin_amdgcn_kernarg_segment_ptr()), "{s2}"(blockIdx.x), "{v0}"(threadIdx.x)
    : "vcc", "scc", "memory", "v1", "v2", "v3", "v4", "v5", "v6", "v7", "v8", "v9", "v10", "v11", "v12", "v13", "v14", "v15", "v16", "v17", "v18", "v19", "v20", "v21", "v22", "v23", "v24", "v25", "v26", "v27", "v28", "v29", "v30", "v31", "v32", "v33", "v34", "v35", "v36", "v37", "v38", "v39", "v40", "v41", "v42", "v43", "v44", "v45", "v46", "v47", "v48", "v49", "v50", "v51", "v52", "v53", "v54", "v55", "v56", "v57", "v58", "v59", "v60", "v61", "v62", "v63", "v64", "v65", "v66", "v67", "v68", "v69", "v70", "v71", "v72", "v73", "v74", "v75", "v76", "v77", "v78", "v79", "v80", "v81", "v82", "v83", "v84", "v85", "v86", "v87", "v88", "v89", "v90", "v91", "v92", "v93", "v94", "v95", "v96", "v97", "v98", "v99", "v100", "v101", "v102", "v103", "v104", "v105", "v106", "v107", "v108", "v109", "v110", "v111", "v112", "v113", "v114", "v115", "v116", "v117", "v118", "v119", "v120", "v121", "v122", "v123", "v124", "v125", "v126", "v127", "v128", "v129", "v130", "v131", "v132", "v133", "v134", "v135", "v136", "v137", "v138", "v139", "v140", "v141", "v142", "v143", "v144", "v145", "v146", "v147", "v148", "v149", "v150", "v151", "v152", "v153", "v154", "v155", "v156", "v157", "v158", "v159", "v160", "v161", "v162", "v163", "v164", "v165", "v166", "v167", "v168", "v169", "v170", "v171", "v172", "v173", "v174", "v175", "v176", "v177", "v178", "v179", "v180", "v181", "v182", "v183", "v184", "v185", "v186", "v187", "v188", "v189", "v190", "v191", "v192", "v193", "v194", "v195", "v196", "v197", "v198", "v199", "v200", "v201", "v202", "v203", "v204", "v205", "v206", "v207", "v208", "v209", "v210", "v211", "v212", "v213", "v214", "v215", "v216", "v217", "v218", "v219", "v220", "v221", "v222", "v223", "v224", "v225", "v226", "v227", "v228", "v229", "v230", "v231", "v232", "v233", "v234", "v235", "v236", "v237", "v238", "v239", "v240", "v241", "v242", "v243", "v244", "v245", "v246", "v247", "v248", "v249", "v250", "v251", "v252", "v253", "v254", "v255", "s3", "s4", "s5", "s6", "s7", "s8", "s9", "s10", "s11", "s12", "s13", "s14", "s15", "s16", "s17", "s18", "s19", "s20", "s21", "s22", "s23", "s24", "s25", "s26", "s27", "s28", "s29", "s30", "s31", "s32", "s33", "s34", "s35", "s36", "s37", "s38", "s39", "s40", "s41", "s42", "s43", "s44", "s45", "s46", "s47", "s48", "s49", "s50", "s51", "s52", "s53", "s54", "s55", "s56", "s57", "s58", "s59", "s60", "s61", "s62", "s63", "s64", "s65", "s66", "s67", "s68", "s69", "s70", "s71", "s72", "s73", "s74", "s75", "s76", "s77", "s78", "s79", "s80", "s81", "s82", "s83", "s84", "s85", "s86", "s87", "s88", "s89", "s90", "s91", "s92", "s93", "s94", "s95", "s96", "s97");
}

extern "C" void kernel_launch(void* const* d_in, const int* in_sizes, int n_in, void* d_out, int out_size, void* d_ws, size_t ws_size,
                              hipStream_t stream) {
  Params p;
  p.x = (const char*)d_in[0]; p.wsr = (const char*)d_in[1]; p.wsi = (const char*)d_in[2]; p.wdr = (const char*)d_in[3];
  p.wdi = (const char*)d_in[4]; p.w1 = (const char*)d_in[6]; p.w2 = (const char*)d_in[7];
  p.out = (char*)d_out; p.ws = (char*)d_ws + WS_OFF;
  (void)hipFuncSetAttribute((const void*)fnet_kernel, hipFuncAttributeMaxDynamicSharedMemorySize, LDS_BYTES);
  fnet_kernel<<<256, 448, LDS_BYTES, stream>>>(p);
}
```
